```python
import torch

def _softmax(x, axis=-1):
    return torch.softmax(x, dim=axis)

def _gelu(x):
    return torch.nn.functional.gelu(x, approximate='tanh')

def _silu(x):
    return torch.nn.functional.silu(x)

def _sigmoid(x):
    return torch.sigmoid(x)

def _layernorm(x, axis=-1, eps=1e-05):
    m = x.mean(dim=axis, keepdim=True)
    v = x.var(dim=axis, keepdim=True, unbiased=False)
    return (x - m) / torch.sqrt(v + eps)

def _scatter_add(tgt, idx, src, axis=0):
    return tgt.clone().index_add_(axis, idx, src)

def _segment_sum(src, seg, n):
    out = torch.zeros((n,) + src.shape[1:], dtype=src.dtype)
    return out.index_add_(0, seg, src)

def _pad(x, pw):
    flat = tuple((v for pair in reversed(pw) for v in pair))
    return torch.nn.functional.pad(x, flat)
kernel_name = 'concat_heads__linear_attn_block__memory_aug_recurrent_mem_tr__prompt'

def setup_inputs():

    _w = lambda a: (lambda t: t.to(torch.bfloat16) if t.is_floating_point() else t)(torch.as_tensor(a))

    def _A_setup():
        B, S, D = (1, 2048, 448)
        return {'x': _w((lambda *s: torch.randn(*s, dtype=torch.bfloat16) * 0.05)(B, S, D)), 'wqkv': _w((lambda *s: torch.randn(*s, dtype=torch.bfloat16) * 1.6)(D, 3 * D)), 'wo': _w((lambda *s: torch.randn(*s, dtype=torch.bfloat16) * 0.1)(D, D))}
    _A_in = _A_setup()

    def _B_setup():
        B, S, Dm = (1, 2048, 448)
        H, Dh = (16, 256)
        M, seg = (8, 32)
        seg = min(seg, S)
        S = S // seg * seg
        return {'x': _w((lambda *s: torch.randn(*s, dtype=torch.bfloat16) * 0.05)(B, S, Dm)), 'mem0': _w((lambda *s: torch.randn(*s, dtype=torch.bfloat16) * 0.8)(B, M, Dm)), 'Wq': _w((lambda *s: torch.randn(*s, dtype=torch.bfloat16) * 1.6)(Dm, H, Dh) * Dm ** (-0.5)), 'Wk': _w((lambda *s: torch.randn(*s, dtype=torch.bfloat16) * 1.6)(Dm, H, Dh) * Dm ** (-0.5)), 'Wv': _w((lambda *s: torch.randn(*s, dtype=torch.bfloat16) * 0.8)(Dm, H, Dh) * Dm ** (-0.5)), 'Wo': _w((lambda *s: torch.randn(*s, dtype=torch.bfloat16) * 0.8)(H, Dh, Dm) * (H * Dh) ** (-0.5)), 'seg_len': seg}
    _B_in = _B_setup()
    return {'A_x': _A_in['x'], 'A_wqkv': _A_in['wqkv'], 'A_wo': _A_in['wo'], 'B_x': _B_in['x'], 'B_mem0': _B_in['mem0'], 'B_Wq': _B_in['Wq'], 'B_Wk': _B_in['Wk'], 'B_Wv': _B_in['Wv'], 'B_Wo': _B_in['Wo'], 'B_seg_len': _B_in['seg_len']}

def reference(A_x, A_wqkv, A_wo, B_x, B_mem0, B_Wq, B_Wk, B_Wv, B_Wo, B_seg_len):

    def _A_ref(x, wqkv, wo):
        B, S, D = x.shape
        H = 16
        Dh = D // H
        qkv = (lambda x, *d: x.permute(*d))((x @ wqkv).reshape(B, S, 3, H, Dh), 2, 0, 3, 1, 4)
        q, k, v = (qkv[0], qkv[1], qkv[2])
        phi = lambda t: torch.where(t > 0, t + 1, torch.exp(t)) + 1e-06
        pq, pk = (phi(q), phi(k))
        kv = torch.einsum('bhsd,bhse->bhde', pk, v)
        z = torch.einsum('bhsd->bhd', pk)
        num = torch.einsum('bhqd,bhde->bhqe', pq, kv)
        den = torch.einsum('bhqd,bhd->bhq', pq, z)[..., None] + 1e-06
        o = num / den
        return (lambda x, *d: x.permute(*d))(o, 0, 2, 1, 3).reshape(B, S, D) @ wo

    def _B_ref(x, mem0, Wq, Wk, Wv, Wo, seg_len):
        B, S, Dm = x.shape
        H, Dh = (Wq.shape[1], Wq.shape[2])
        M = mem0.shape[1]
        scale = Dh ** (-0.5)
        n_seg = S // seg_len
        L = M + seg_len
        idx = torch.arange(L)
        mask = (idx[:, None] < M) | (idx[:, None] >= idx[None, :])
        mem = mem0
        outs = []
        for s in range(n_seg):
            xs = x[:, s * seg_len:(s + 1) * seg_len, :].clone()
            h = torch.concatenate([mem, xs], axis=1)
            q = torch.einsum('btd,dhe->bhte', h, Wq)
            k = torch.einsum('btd,dhe->bhte', h, Wk)
            v = torch.einsum('btd,dhe->bhte', h, Wv)
            scores = torch.einsum('bhqe,bhke->bhqk', q, k) * scale
            scores = torch.where(mask[None, None], scores, -1000000000.0)
            p = _softmax(scores, axis=-1)
            o = torch.einsum('bhqk,bhke->bhqe', p, v)
            o = torch.einsum('bhte,hed->btd', o, Wo)
            mem = o[:, :M, :].clone()
            outs.append(o[:, M:, :])
        return torch.concatenate(outs, axis=1)
    a = _A_ref(A_x, A_wqkv, A_wo)
    b = _B_ref(B_x, B_mem0, B_Wq, B_Wk, B_Wv, B_Wo, B_seg_len)
    return torch.concatenate([a, b], axis=-1)
```

```cpp
#include <hip/hip_runtime.h>
#include <hip/hip_bf16.h>
typedef __hip_bfloat16 bf16;
__device__ inline float b2f(bf16 x){return __bfloat162float(x);}
__device__ inline bf16 f2b(float x){return __float2bfloat16(x);}
__global__ void gemm_naive(const bf16* A,int lda,const bf16* B,int ldb,bf16* C,int ldc,int M,int N,int K){
  __shared__ float As[16][17], Bs[16][17];
  int tx=threadIdx.x&15, ty=threadIdx.x>>4;
  int row=blockIdx.y*16+ty, col=blockIdx.x*16+tx;
  float acc=0;
  for(int k0=0;k0<K;k0+=16){
    As[ty][tx]=(row<M)?b2f(A[(size_t)row*lda+k0+tx]):0.f;
    Bs[ty][tx]=(col<N)?b2f(B[(size_t)(k0+ty)*ldb+col]):0.f;
    __syncthreads();
    for(int k=0;k<16;k++) acc+=As[ty][k]*Bs[k][tx];
    __syncthreads();
  }
  if(row<M&&col<N) C[(size_t)row*ldc+col]=f2b(acc);
}
__device__ inline float phi(float t){ float r = t>0? b2f(f2b(t+1.f)) : b2f(f2b(expf(t))); return b2f(f2b(r+1e-6f)); }
__global__ void kv_kernel(const bf16* qkv, float* kv, float* z){
  int h=blockIdx.x; int t=threadIdx.x;
  int d=t/29, e=t%29; if(d>=28) return;
  float acc=0;
  for(int s=0;s<2048;s++){
    float pk=phi(b2f(qkv[(size_t)s*1344+448+h*28+d]));
    float v= e<28? b2f(qkv[(size_t)s*1344+896+h*28+e]) : 1.f;
    acc+=pk*v;
  }
  acc=b2f(f2b(acc));
  if(e<28) kv[(h*28+d)*28+e]=acc; else z[h*28+d]=acc;
}
__global__ void o_kernel(const bf16* qkv,const float* kv,const float* z,bf16* o){
  int s=blockIdx.x; int c=threadIdx.x; if(c>=448) return;
  int h=c/28,e=c%28; float num=0,den=0;
  for(int d=0;d<28;d++){ float pq=phi(b2f(qkv[(size_t)s*1344+h*28+d])); num+=pq*kv[(h*28+d)*28+e]; den+=pq*z[h*28+d]; }
  num=b2f(f2b(num)); den=b2f(f2b(b2f(f2b(den))+1e-6f));
  o[(size_t)s*448+c]=f2b(num/den);
}
__global__ void attn_kernel(const bf16* Qx,const bf16* Kx,const bf16* Vx,const bf16* qm,const bf16* km,const bf16* vm,bf16* oatt,int seg){
  int h=blockIdx.x, r=blockIdx.y, t=threadIdx.x;
  __shared__ float p[40];
  const bf16* q = r<8? qm+(size_t)r*4096+h*256 : Qx+(size_t)(seg*32+r-8)*4096+h*256;
  if(t<40){
    const bf16* k = t<8? km+(size_t)t*4096+h*256 : Kx+(size_t)(seg*32+t-8)*4096+h*256;
    float s=0; for(int e=0;e<256;e++) s+=b2f(q[e])*b2f(k[e]);
    s=b2f(f2b(s))*0.0625f;
    if(!(r<8||r>=t)) s=-1e9f;
    p[t]=s;
  }
  __syncthreads();
  if(t==0){ float m=-1e30f; for(int j=0;j<40;j++) m=fmaxf(m,p[j]); float sum=0; for(int j=0;j<40;j++){p[j]=expf(p[j]-m); sum+=p[j];} for(int j=0;j<40;j++) p[j]=b2f(f2b(p[j]/sum)); }
  __syncthreads();
  for(int e=t;e<256;e+=64){ float a=0; for(int j=0;j<40;j++){ const bf16* v = j<8? vm+(size_t)j*4096+h*256 : Vx+(size_t)(seg*32+j-8)*4096+h*256; a+=p[j]*b2f(v[e]);} oatt[(size_t)r*4096+h*256+e]=f2b(a);}
}
extern "C" void kernel_launch(void* const* d_in, const int* in_sizes, int n_in, void* d_out, int out_size, void* d_ws, size_t ws_size, hipStream_t stream){
  const bf16* Ax=(const bf16*)d_in[0]; const bf16* Awqkv=(const bf16*)d_in[1]; const bf16* Awo=(const bf16*)d_in[2];
  const bf16* Bx=(const bf16*)d_in[3]; const bf16* mem0=(const bf16*)d_in[4]; const bf16* Wq=(const bf16*)d_in[5]; const bf16* Wk=(const bf16*)d_in[6]; const bf16* Wv=(const bf16*)d_in[7]; const bf16* Wo=(const bf16*)d_in[8];
  bf16* out=(bf16*)d_out;
  char* ws=(char*)d_ws; size_t off=0;
  auto alloc=[&](size_t b){ void* p=ws+off; off+=(b+255)/256*256; return p; };
  bf16* qkv=(bf16*)alloc(2048*1344*2); float* kv=(float*)alloc(16*28*28*4); float* z=(float*)alloc(448*4); bf16* o=(bf16*)alloc(2048*448*2);
  bf16* Qx=(bf16*)alloc((size_t)2048*4096*2); bf16* Kx=(bf16*)alloc((size_t)2048*4096*2); bf16* Vx=(bf16*)alloc((size_t)2048*4096*2);
  bf16* qm=(bf16*)alloc(8*4096*2); bf16* km=(bf16*)alloc(8*4096*2); bf16* vm=(bf16*)alloc(8*4096*2); bf16* oatt=(bf16*)alloc(40*4096*2); bf16* mem=(bf16*)alloc(8*448*2);
  gemm_naive<<<dim3(1344/16,2048/16),256,0,stream>>>(Ax,448,Awqkv,1344,qkv,1344,2048,1344,448);
  kv_kernel<<<16,832,0,stream>>>(qkv,kv,z);
  o_kernel<<<2048,448,0,stream>>>(qkv,kv,z,o);
  gemm_naive<<<dim3(448/16,2048/16),256,0,stream>>>(o,448,Awo,448,out,896,2048,448,448);
  gemm_naive<<<dim3(4096/16,2048/16),256,0,stream>>>(Bx,448,Wq,4096,Qx,4096,2048,4096,448);
  gemm_naive<<<dim3(4096/16,2048/16),256,0,stream>>>(Bx,448,Wk,4096,Kx,4096,2048,4096,448);
  gemm_naive<<<dim3(4096/16,2048/16),256,0,stream>>>(Bx,448,Wv,4096,Vx,4096,2048,4096,448);
  for(int s=0;s<64;s++){
    const bf16* m = s==0? mem0 : mem;
    gemm_naive<<<dim3(4096/16,1),256,0,stream>>>(m,448,Wq,4096,qm,4096,8,4096,448);
    gemm_naive<<<dim3(4096/16,1),256,0,stream>>>(m,448,Wk,4096,km,4096,8,4096,448);
    gemm_naive<<<dim3(4096/16,1),256,0,stream>>>(m,448,Wv,4096,vm,4096,8,4096,448);
    attn_kernel<<<dim3(16,40),64,0,stream>>>(Qx,Kx,Vx,qm,km,vm,oatt,s);
    gemm_naive<<<dim3(448/16,1),256,0,stream>>>(oatt,4096,Wo,448,mem,448,8,448,4096);
    gemm_naive<<<dim3(448/16,2),256,0,stream>>>(oatt+8*4096,4096,Wo,448,out+(size_t)s*32*896+448,896,32,448,4096);
  }
}
```

```cpp
#include <hip/hip_runtime.h>
#include <hip/hip_bf16.h>
#include <stdint.h>

typedef unsigned u32;
typedef u32 u2 __attribute__((ext_vector_type(2)));
typedef u32 u4 __attribute__((ext_vector_type(4)));
typedef u32 u8v __attribute__((ext_vector_type(8)));
typedef float f2 __attribute__((ext_vector_type(2)));
typedef float f4 __attribute__((ext_vector_type(4)));
typedef __bf16 b2 __attribute__((ext_vector_type(2)));
typedef __bf16 b8 __attribute__((ext_vector_type(8)));
typedef __bf16 b16v __attribute__((ext_vector_type(16)));
typedef __amdgpu_buffer_rsrc_t rsrc_t;

#define DEVI __device__ __forceinline__
constexpr int SC1 = 16;
constexpr int D = 448, NSEG = 64;

constexpr u32 MB2 = 2u << 20;
constexpr u32 OFF_MEMA = 0;
constexpr u32 OFF_P    = MB2;
constexpr u32 OFF_MEMN = 2 * MB2;
constexpr u32 OFF_PX   = OFF_MEMN + 64 * 7168;
constexpr u32 OFF_KVF  = OFF_PX + 64 * 40960;
constexpr u32 OFF_FLAG = OFF_KVF + 32768;
constexpr u32 OFF_INIT_END = OFF_FLAG + 4096;
constexpr u32 OFF_WA   = 4 * MB2;
constexpr u32 WSZ      = 16 * 28 * 14 * 1024;
constexpr u32 OFF_WB   = OFF_WA + WSZ;
constexpr u32 OFF_WQKV = OFF_WB + WSZ;
constexpr u32 OFF_WO   = OFF_WQKV + 1344 * 1024;
constexpr u32 OFF_KVP  = OFF_WO + 448 * 1024;
constexpr u32 OFF_DBG  = OFF_KVP + 32 * 16 * 4096;
constexpr u32 WS_USED  = OFF_DBG + 65536;
static_assert(OFF_INIT_END <= OFF_WA, "layout");

constexpr int SPIN_MAX = 1 << 17;

DEVI rsrc_t mk(const void* p, u32 bytes) { return __builtin_amdgcn_make_buffer_rsrc((void*)p, (short)0, (int)bytes, 0x00020000); }
DEVI u4 ld16(rsrc_t r, u32 v, u32 s) { return __builtin_amdgcn_raw_buffer_load_b128(r, v, s, 0); }
DEVI u4 ld16c(rsrc_t r, u32 v, u32 s) { return __builtin_amdgcn_raw_buffer_load_b128(r, v, s, SC1); }
DEVI void st16(rsrc_t r, u32 v, u32 s, u4 x) { __builtin_amdgcn_raw_buffer_store_b128(x, r, v, s, 0); }
DEVI void st16c(rsrc_t r, u32 v, u32 s, u4 x) { __builtin_amdgcn_raw_buffer_store_b128(x, r, v, s, SC1); }
DEVI void st8c(rsrc_t r, u32 v, u32 s, u2 x) { __builtin_amdgcn_raw_buffer_store_b64(x, r, v, s, SC1); }
DEVI void st4c(rsrc_t r, u32 v, u32 s, u32 x) { __builtin_amdgcn_raw_buffer_store_b32(x, r, v, s, SC1); }
DEVI u32 pk(float a, float b) { f2 t = {a, b}; return __builtin_bit_cast(u32, __builtin_convertvector(t, b2)); }
DEVI u32 nosent(u32 x) { return x < 0xFFFFFFFEu ? x : 0xFFFFFFFEu; }
DEVI f4 mfma(u4 a, u4 b, f4 c) { return __builtin_amdgcn_mfma_f32_16x16x32_bf16(__builtin_bit_cast(b8, a), __builtin_bit_cast(b8, b), c, 0, 0, 0); }
template <int AB> DEVI f4 smf(u4 a, u8v b, f4 c, int idx) { return __builtin_amdgcn_smfmac_f32_16x16x64_bf16(__builtin_bit_cast(b8, a), __builtin_bit_cast(b16v, b), c, idx, 0, AB); }
DEVI u8v cat(u4 a, u4 b) { return __builtin_shufflevector(a, b, 0, 1, 2, 3, 4, 5, 6, 7); }
DEVI u4 lo4(u8v a) { return __builtin_shufflevector(a, a, 0, 1, 2, 3); }
DEVI u4 hi4(u8v a) { return __builtin_shufflevector(a, a, 4, 5, 6, 7); }
DEVI u32 umax3(u32 a, u32 b, u32 c) { u32 m = a > b ? a : b; return m > c ? m : c; }
DEVI u32 umax4(u4 a) { u32 m = umax3(a[0], a[1], a[2]); return m > a[3] ? m : a[3]; }
DEVI bool any_sent(u32 m) { return __builtin_amdgcn_ballot_w64(m == 0xFFFFFFFFu) != 0; }
DEVI void cbar() { asm volatile("" ::: "memory"); }
DEVI void lds_bar() { asm volatile("s_waitcnt lgkmcnt(0)\n\ts_barrier" ::: "memory"); }
template <int C> DEVI float dppf(float x) { return __builtin_bit_cast(float, __builtin_amdgcn_update_dpp(0, __builtin_bit_cast(int, x), C, 0xf, 0xf, true)); }

DEVI int colmap(int w, int tt, int m) {
  int q = m >> 2, r = m & 3, b = 112 * w + 4 * (r >> 1) + (r & 1);
  if (tt < 4) return b + 16 * q + 8 * (tt >> 1) + 2 * (tt & 1);
  if (tt < 6) return b + 64 + 8 * q + 2 * (tt - 4);
  return b + 96 + 8 * (q >> 1) + 2 * (q & 1);
}

__global__ void __launch_bounds__(256, 1)
prologue(const void* wqkv, const void* wo, const void* mem0, const void* Wq, const void* Wk, const void* Wv, const void* Wo, char* ws) {
  __shared__ __attribute__((aligned(16))) u4 lds[7 * 8 * 64];
  const int g = blockIdx.x, tid = threadIdx.x, l = tid & 63, w = __builtin_amdgcn_readfirstlane(tid >> 6);
  const int id = (g & 7) * 4 + (g >> 6), piece = (g >> 3) & 7;
  const int mat = id & 1, h = id >> 1, rh = piece >> 2, cq = piece & 3;
  const int n = l & 15, q = l >> 4;
  const u32 WBYTES = 448u * 4096u * 2u;
  rsrc_t rw = mk(ws, WS_USED);
  rsrc_t ra = mk(mat ? Wv : Wq, WBYTES);
  u4 af[4][8];
  const int nk = (w == 3) ? 1 : 2;
#pragma unroll
  for (int mt = 0; mt < 4; ++mt) {
    int ks = 7 * rh + 2 * w + (mt >> 1);
    int k = 32 * ks + 8 * (n >> 2) + 4 * (mt & 1) + (n & 3);
    u32 off = (u32)k * 8192u + (u32)h * 512u + q * 16;
    if ((mt >> 1) >= nk) off = 0x80000000u;
#pragma unroll
    for (int es = 0; es < 8; ++es) af[mt][es] = ld16(ra, off + es * 64, 0);
  }
  if (mat == 0) {
    rsrc_t rb = mk(Wk, WBYTES);
#pragma unroll
    for (int i = 0; i < 14; ++i) {
      int f = w * 14 + i, tt = f >> 3, es = f & 7;
      int c = colmap(cq, tt, n);
      lds[f * 64 + l] = ld16(rb, (u32)c * 8192u + (u32)h * 512u + es * 64 + q * 16, 0);
    }
  } else {
    rsrc_t rb = mk(Wo, WBYTES);
#pragma unroll
    for (int i = 0; i < 2; ++i) {
      int it = tid + 256 * i;
      if (it < 448) {
        int eg = it / 14, ch = it - eg * 14;
        u4 r[8];
#pragma unroll
        for (int j = 0; j < 8; ++j) r[j] = ld16(rb, (u32)(h * 256 + eg * 8 + j) * 896u + cq * 224 + ch * 16, 0);
        int tt = ch >> 1, es = eg >> 2, qq = eg & 3;
#pragma unroll
        for (int c = 0; c < 8; ++c) {
          u32 sel = (c & 1) ? 0x07060302u : 0x05040100u;
          u4 o;
#pragma unroll
          for (int d = 0; d < 4; ++d) o[d] = __builtin_amdgcn_perm(r[2 * d + 1][c >> 1], r[2 * d][c >> 1], sel);
          lds[(tt * 8 + es) * 64 + qq * 16 + (ch & 1) * 8 + c] = o;
        }
      }
    }
  }
  {
    u4 ones = {~0u, ~0u, ~0u, ~0u};
    for (u32 o = (u32)g * 4096u + tid * 16; o < (1u << 20); o += 256u * 4096u) if (o >= 16384u) st16(rw, OFF_MEMA + o, 0, ones);
    for (u32 o = (u32)g * 4096u + tid * 16; o < 63u * 32768u; o += 256u * 4096u) st16(rw, OFF_P + o, 0, ones);
    for (u32 o = OFF_MEMN + 7168u + (u32)g * 4096u + tid * 16; o < OFF_INIT_END; o += 256u * 4096u) st16(rw, o, 0, ones);
    if (g == 0) {
      const u32* m = (const u32*)mem0;
      for (int i = tid; i < 1792; i += 256) {
        int kb = i >> 8, ll = (i >> 2) & 63, d = i & 3, rr = ll & 15, qq = ll >> 4;
        u32 v = nosent(m[(rr & 7) * 224 + 32 * kb + 8 * qq + 2 * d + (rr >> 3)]);
        ((u32*)(ws + OFF_MEMA))[i] = v;
        ((u32*)(ws + OFF_MEMA + 8192))[i] = v;
        ((u32*)(ws + OFF_MEMN))[i] = nosent(m[i]);
      }
    }
  }
  if (w == 3) {
    for (int i = 0; i < 7; ++i) {
      int f = g * 7 + i;
      u32 v[8];
      if (f < 1344) {
        int ks = f % 14, t = f / 14, ct = t & 1, hh = (t >> 1) & 15, m3 = t >> 5;
        int dd = 16 * ct + n;
        const unsigned short* p = (const unsigned short*)wqkv + (size_t)(32 * ks + 8 * q) * 1344 + m3 * 448 + hh * 28 + (dd < 28 ? dd : 0);
#pragma unroll
        for (int j = 0; j < 8; ++j) v[j] = dd < 28 ? p[j * 1344] : 0;
      } else {
        int t = f - 1344, hh = t & 15, ct = t >> 4;
#pragma unroll
        for (int j = 0; j < 8; ++j) {
          int e = 16 * (j >> 2) + 4 * q + (j & 3);
          v[j] = e < 28 ? ((const unsigned short*)wo)[(size_t)(hh * 28 + e) * 448 + 16 * ct + n] : 0;
        }
      }
      u4 o = {v[0] | v[1] << 16, v[2] | v[3] << 16, v[4] | v[5] << 16, v[6] | v[7] << 16};
      st16(rw, OFF_WQKV + (u32)f * 1024u + l * 16, 0, o);
    }
  }
  __syncthreads();
  f4 acc[4][7];
#pragma unroll
  for (int mt = 0; mt < 4; ++mt)
#pragma unroll
    for (int tt = 0; tt < 7; ++tt) acc[mt][tt] = (f4){0, 0, 0, 0};
  if (w == 3) {
#pragma unroll
    for (int es = 0; es < 8; ++es)
#pragma unroll
      for (int tt = 0; tt < 7; ++tt) {
        u4 b = lds[(tt * 8 + es) * 64 + l];
        acc[0][tt] = mfma(af[0][es], b, acc[0][tt]);
        acc[1][tt] = mfma(af[1][es], b, acc[1][tt]);
      }
  } else {
#pragma unroll
    for (int es = 0; es < 8; ++es)
#pragma unroll
      for (int tt = 0; tt < 7; ++tt) {
        u4 b = lds[(tt * 8 + es) * 64 + l];
#pragma unroll
        for (int mt = 0; mt < 4; ++mt) acc[mt][tt] = mfma(af[mt][es], b, acc[mt][tt]);
      }
  }
  const float sc = mat ? 1.0f : 0.0625f * 1.4426950408889634f;
#pragma unroll
  for (int kk = 0; kk < 2; ++kk) {
    if (kk < nk) {
      int ks = 7 * rh + 2 * w + kk;
#pragma unroll
      for (int tt = 0; tt < 7; ++tt) {
        f4 a = acc[2 * kk][tt] * sc, b = acc[2 * kk + 1][tt] * sc;
        u4 o = {pk(a[0], a[1]), pk(a[2], a[3]), pk(b[0], b[1]), pk(b[2], b[3])};
        u32 fr = mat ? (u32)(((7 * cq + tt) * 16 + h) * 14 + ks) : (u32)(((h * 4 + cq) * 7 + tt) * 14 + ks);
        st16(rw, (mat ? OFF_WB : OFF_WA) + fr * 1024u + l * 16, 0, o);
      }
    }
  }
}

constexpr int LDS_BYTES = 128 * 1024;

struct Ctx {
  rsrc_t rw, rx, ro;
  char* lds;
  int l, w, n, q;
  u32 parM, parP;
};

DEVI void role_S(const Ctx& c, int h) {
  const int l = c.l, w = c.w, n = c.n, q = c.q;
  char* L = c.lds;
  constexpr int L_W6 = 0, L_XK = 57344, L_MEM = L_XK + 32768, L_SC = L_MEM + 8192;
  u8v W[6][7];
#pragma unroll
  for (int i = 0; i < 7; ++i) {
    int kb = 2 * w + i; kb = kb >= 7 ? kb - 7 : kb;
#pragma unroll
    for (int tt = 0; tt < 7; ++tt) {
      u32 so = OFF_WA + (u32)(((h * 4 + w) * 7 + tt) * 14 + 2 * kb) * 1024u;
      u4 a = ld16(c.rw, l * 16, so), b = ld16(c.rw, l * 16 + 1024, so);
      if (tt < 6) W[tt][i] = cat(a, b);
      else { *(u4*)(L + L_W6 + ((w * 7 + i) * 2 + 0) * 1024 + l * 16) = a; *(u4*)(L + L_W6 + ((w * 7 + i) * 2 + 1) * 1024 + l * 16) = b; }
    }
  }
  u4 SEL;
#pragma unroll
  for (int d = 0; d < 4; ++d) {
    u32 v = 0;
#pragma unroll
    for (int e = 0; e < 2; ++e) { int j = 2 * d + e; if ((n & 7) == ((4 * q + (j & 3)) & 7) && (n >> 3) == (j >> 2)) v |= 0x3F80u << (16 * e); }
    SEL[d] = v;
  }
  const int idx = (n < 8 ? 0x00444444 : 0x00EEEEEE) | ((q & 1) ? 0xEE000000 : 0x44000000);
  int kc[4];
  kc[0] = 112 * w + 8 * q; kc[1] = kc[0] + 32;
  kc[2] = 112 * w + ((q & 1) ? 96 : 64 + 4 * q);
  kc[3] = 112 * w + ((q & 1) ? 104 : 80 + 4 * q);
  u32 xoff[4], mko[4];
#pragma unroll
  for (int i = 0; i < 4; ++i) {
    xoff[i] = (u32)n * 896u + kc[i] * 2;
    int col = kc[i];
    mko[i] = L_MEM + (col >> 6) * 1024 + ((col >> 4) & 3) * 256 + (n & 7) * 16 + ((col >> 3) & 1) * 8;
  }
  const int kb1 = (w == 3) ? 0 : 2 * w + 1;
  const u32 wsc = L_SC + ((2 * w + (q >> 1)) * 416 + 4 * (q & 1) * 52) * 4;
  const u32 pos2 = (n < 8 ? n : 32 + n) * 4;
  const int slot = l & 31, row = 2 * w + (l >> 5);
  const u32 rsc = L_SC + (row * 52 + 2 * (slot < 20 ? slot : 0)) * 4;
  const u32 pst = slot < 20 ? (u32)((h >> 3) * 16384 + (h & 7) * 640 + row * 80 + slot * 4) : 0x80000000u;

  for (int s = 0; s < NSEG - 1; ++s) {
    {
      u4 xk[8];
#pragma unroll
      for (int i = 0; i < 8; ++i) xk[i] = ld16(c.rx, xoff[i & 3] + (i >> 2) * 16 * 896, (u32)s * 32u * 896u);
#pragma unroll
      for (int i = 0; i < 8; ++i) *(u4*)(L + L_XK + (w * 8 + i) * 1024 + l * 16) = xk[i];
    }
    u8v w60 = cat(*(u4*)(L + L_W6 + (w * 14 + 0) * 1024 + l * 16), *(u4*)(L + L_W6 + (w * 14 + 1) * 1024 + l * 16));
    u8v w61 = cat(*(u4*)(L + L_W6 + (w * 14 + 2) * 1024 + l * 16), *(u4*)(L + L_W6 + (w * 14 + 3) * 1024 + l * 16));
    u4 A[7];
    const u32 sm = OFF_MEMA + (u32)s * 16384u + c.parM;
    for (int sp = 0; sp < SPIN_MAX; ++sp) {
      cbar();
      A[0] = ld16c(c.rw, 2 * w * 1024 + l * 16, sm);
      A[1] = ld16c(c.rw, kb1 * 1024 + l * 16, sm);
      if (!any_sent(umax3(umax4(A[0]), umax3(A[1][0], A[1][1], A[1][2]), A[1][3]))) break;
    }
    *(u4*)(L + L_MEM + 2 * w * 1024 + l * 16) = A[0];
    *(u4*)(L + L_MEM + (2 * w + 1) * 1024 + l * 16) = A[1];
    f4 acc[7];
    const f4 z = {0, 0, 0, 0};
#pragma unroll
    for (int tt = 0; tt < 6; ++tt) acc[tt] = smf<0>(A[0], W[tt][0], z, idx);
    acc[6] = smf<0>(A[0], w60, z, idx);
    lds_bar();
#pragma unroll
    for (int i = 2; i < 7; ++i) {
      int kb = 2 * w + i; kb = kb >= 7 ? kb - 7 : kb;
      A[i] = *(u4*)(L + L_MEM + kb * 1024 + l * 16);
    }
#pragma unroll
    for (int tt = 0; tt < 6; ++tt) acc[tt] = smf<0>(A[1], W[tt][1], acc[tt], idx);
    acc[6] = smf<0>(A[1], w61, acc[6], idx);
    f4 sc[3];
#pragma unroll
    for (int tt = 0; tt < 4; ++tt)
#pragma unroll
      for (int i = 2; i < 7; ++i) acc[tt] = smf<0>(A[i], W[tt][i], acc[tt], idx);
    u4 ta = {pk(acc[0][0], acc[0][1]), pk(acc[0][2], acc[0][3]), pk(acc[1][0], acc[1][1]), pk(acc[1][2], acc[1][3])};
    u4 tb = {pk(acc[2][0], acc[2][1]), pk(acc[2][2], acc[2][3]), pk(acc[3][0], acc[3][1]), pk(acc[3][2], acc[3][3])};
#pragma unroll
    for (int i = 2; i < 7; ++i) acc[4] = smf<0>(A[i], W[4][i], acc[4], idx);
    f4 da = mfma(ta, SEL, z), db = mfma(tb, SEL, z);
#pragma unroll
    for (int i = 2; i < 7; ++i) acc[5] = smf<0>(A[i], W[5][i], acc[5], idx);
    {
      u4 t1 = {pk(da[0], da[1]), pk(da[2], da[3]), pk(db[0], db[1]), pk(db[2], db[3])};
#pragma unroll
      for (int kt = 0; kt < 2; ++kt) {
        u8v k = cat(*(u4*)(L + L_XK + (w * 8 + kt * 4 + 0) * 1024 + l * 16), *(u4*)(L + L_XK + (w * 8 + kt * 4 + 1) * 1024 + l * 16));
        sc[kt] = smf<0>(t1, k, z, idx);
      }
      u8v k;
#pragma unroll
      for (int i = 0; i < 2; ++i) {
        const u32* p = (const u32*)(L + mko[i]);
        k[4 * i + 0] = p[0]; k[4 * i + 1] = p[32]; k[4 * i + 2] = p[1]; k[4 * i + 3] = p[33];
      }
      sc[2] = smf<0>(t1, k, z, idx);
    }
    u4 tc = {pk(acc[4][0], acc[4][1]), pk(acc[4][2], acc[4][3]), pk(acc[5][0], acc[5][1]), pk(acc[5][2], acc[5][3])};
#pragma unroll
    for (int i = 2; i < 7; ++i) {
      u8v w6 = cat(*(u4*)(L + L_W6 + (w * 14 + 2 * i) * 1024 + l * 16), *(u4*)(L + L_W6 + (w * 14 + 2 * i + 1) * 1024 + l * 16));
      acc[6] = smf<0>(A[i], w6, acc[6], idx);
      if (i == 3) da = mfma(tc, SEL, z);
    }
    u4 td = {pk(acc[6][0], acc[6][1]), pk(acc[6][2], acc[6][3]), 0, 0};
    db = mfma(td, SEL, z);
    {
      u4 t2 = {pk(da[0], da[1]), pk(da[2], da[3]), pk(db[0], db[1]), pk(db[2], db[3])};
#pragma unroll
      for (int kt = 0; kt < 2; ++kt) {
        u8v k = cat(*(u4*)(L + L_XK + (w * 8 + kt * 4 + 2) * 1024 + l * 16), *(u4*)(L + L_XK + (w * 8 + kt * 4 + 3) * 1024 + l * 16));
        sc[kt] = smf<1>(t2, k, sc[kt], idx);
      }
      u8v k;
#pragma unroll
      for (int i = 0; i < 2; ++i) {
        const u32* p = (const u32*)(L + mko[2 + i]);
        k[4 * i + 0] = p[0]; k[4 * i + 1] = p[32]; k[4 * i + 2] = p[1]; k[4 * i + 3] = p[33];
      }
      sc[2] = smf<1>(t2, k, sc[2], idx);
    }
#pragma unroll
    for (int r = 0; r < 4; ++r) {
      *(float*)(L + wsc + r * 208 + (8 + n) * 4) = sc[0][r];
      *(float*)(L + wsc + r * 208 + (24 + n) * 4) = sc[1][r];
      *(float*)(L + wsc + r * 208 + pos2) = sc[2][r];
    }
    lds_bar();
    f2 v[8];
#pragma unroll
    for (int p = 0; p < 8; ++p) v[p] = *(f2*)(L + rsc + p * 1664);
    f2 sv = ((v[0] + v[1]) + (v[2] + v[3])) + ((v[4] + v[5]) + (v[6] + v[7]));
    if (slot >= 20) sv = (f2){-INFINITY, -INFINITY};
    float m = fmaxf(sv[0], sv[1]);
    m = fmaxf(m, dppf<0x128>(m)); m = fmaxf(m, dppf<0x124>(m)); m = fmaxf(m, dppf<0x122>(m)); m = fmaxf(m, dppf<0x121>(m));
    { auto r = __builtin_amdgcn_permlane16_swap(__builtin_bit_cast(u32, m), __builtin_bit_cast(u32, m), false, false);
      u32 r0 = r[0], r1 = r[1]; m = fmaxf(__builtin_bit_cast(float, r0), __builtin_bit_cast(float, r1)); }
    float e0 = __builtin_amdgcn_exp2f(sv[0] - m), e1 = __builtin_amdgcn_exp2f(sv[1] - m);
    float t = e0 + e1;
    t += dppf<0x128>(t); t += dppf<0x124>(t); t += dppf<0x122>(t); t += dppf<0x121>(t);
    { auto r = __builtin_amdgcn_permlane16_swap(__builtin_bit_cast(u32, t), __builtin_bit_cast(u32, t), false, false);
      u32 r0 = r[0], r1 = r[1]; t = __builtin_bit_cast(float, r0) + __builtin_bit_cast(float, r1); }
    float inv = __builtin_amdgcn_rcpf(t);
    st4c(c.rw, pst, OFF_P + (u32)s * 32768u + c.parP, nosent(pk(e0 * inv, e1 * inv)));
  }
}

DEVI void role_V(const Ctx& c, int j) {
  const int l = c.l, w = c.w, n = c.n, q = c.q;
  char* L = c.lds;
  constexpr int L_X = 0, L_RO = 28672, L_RC = L_RO + 4 * 8 * 8 * 80;
  u8v W[4][7];
#pragma unroll
  for (int hh = 0; hh < 4; ++hh)
#pragma unroll
    for (int kb = 0; kb < 7; ++kb) {
      u32 so = OFF_WB + (u32)((j * 16 + 4 * w + hh) * 14 + 2 * kb) * 1024u;
      W[hh][kb] = cat(ld16(c.rw, l * 16, so), ld16(c.rw, l * 16 + 1024, so));
    }
  const int idx = n < 8 ? 0x4444 : 0xEEEE;
  u32 xoff[7];
#pragma unroll
  for (int i = 0; i < 7; ++i) { int f = 7 * w + i, mt = f >= 14, ks = f - 14 * mt; xoff[i] = (u32)(16 * mt + n) * 896u + ks * 64 + q * 16; }
  {
    u4 xr[7];
#pragma unroll
    for (int i = 0; i < 7; ++i) xr[i] = ld16(c.rx, xoff[i], 0);
#pragma unroll
    for (int i = 0; i < 7; ++i) *(u4*)(L + L_X + (7 * w + i) * 1024 + l * 16) = xr[i];
  }
  int ph[3]; u32 pin[3];
  ph[0] = 4 * w + (n >> 3); ph[1] = ph[0] + 2; ph[2] = 4 * w + 2 * (q >> 1) + (n >> 3);
  pin[0] = pin[1] = (n & 7) * 80 + 16 + q * 16; pin[2] = (n & 7) * 80;
  u32 po[3], pxo[3];
#pragma unroll
  for (int i = 0; i < 3; ++i) { po[i] = (ph[i] >> 3) * 16384 + (ph[i] & 7) * 640 + pin[i]; pxo[i] = ph[i] * 2560 + pin[i]; }
  const u32 wr = ((2 * w + (q >> 1)) * 8 + 4 * (q & 1)) * 80 + n * 4;
  const int rp = l >> 2, dd = l & 3, ccol = 4 * dd + 2 * (rp >> 3);
  const u32 rdc = L_RC + (rp & 7) * 80 + ccol * 4;
  const u32 stA = (j >> 2) * 1024 + (j & 3) * 256 + l * 4;
  const u32 stN = (rp & 7) * 896 + (16 * j + ccol) * 2;
  const u32 rdo = L_RO + w * 5120 + (l >> 3) * 80 + (l & 7) * 8;
  const u32 sto = (u32)(8 * w + (l >> 3)) * 1792u + 896 + j * 32 + (l & 7) * 4;
  lds_bar();
  u8v Y[3];
  const f4 z = {0, 0, 0, 0};
  for (int s = 0; s <= NSEG; ++s) {
    u4 xr[7], px[12];
    if (s + 1 < NSEG) {
#pragma unroll
      for (int i = 0; i < 7; ++i) xr[i] = ld16(c.rx, xoff[i], (u32)(s + 1) * 32u * 896u);
    }
    f4 acc[4][2];
    if (s < NSEG) {
#pragma unroll
      for (int hh = 0; hh < 4; ++hh) acc[hh][0] = acc[hh][1] = z;
#pragma unroll
      for (int ks = 0; ks < 14; ++ks) {
        u4 a0 = *(u4*)(L + L_X + ks * 1024 + l * 16), a1 = *(u4*)(L + L_X + (14 + ks) * 1024 + l * 16);
#pragma unroll
        for (int hh = 0; hh < 4; ++hh) {
          u4 b = (ks & 1) ? hi4(W[hh][ks >> 1]) : lo4(W[hh][ks >> 1]);
          acc[hh][0] = mfma(a0, b, acc[hh][0]);
          acc[hh][1] = mfma(a1, b, acc[hh][1]);
        }
      }
    }
    if (s > 0) {
      const u32 so = OFF_PX + (u32)(s - 1) * 40960u;
      for (int sp = 0; sp < SPIN_MAX; ++sp) {
        cbar();
        u32 m = 0;
#pragma unroll
        for (int i = 0; i < 12; ++i) px[i] = ld16c(c.rw, pxo[i % 3] + (i / 3) * 640, so);
#pragma unroll
        for (int i = 0; i < 12; ++i) m = umax3(umax3(m, px[i][0], px[i][1]), px[i][2], px[i][3]);
        if (!any_sent(m)) break;
        __builtin_amdgcn_s_sleep(2);
      }
#pragma unroll
      for (int g = 0; g < 4; ++g) {
        f4 o = smf<0>(px[3 * g], Y[0], z, idx);
        o = smf<0>(px[3 * g + 1], Y[1], o, idx);
        o = smf<0>(px[3 * g + 2], Y[2], o, idx);
#pragma unroll
        for (int r = 0; r < 4; ++r) *(float*)(L + L_RO + g * 5120 + wr + r * 80) = o[r];
      }
    }
    lds_bar();
    if (s > 0) {
      f2 v[8];
#pragma unroll
      for (int p = 0; p < 8; ++p) v[p] = *(f2*)(L + rdo + p * 640);
      f2 sv = ((v[0] + v[1]) + (v[2] + v[3])) + ((v[4] + v[5]) + (v[6] + v[7]));
      __builtin_amdgcn_raw_buffer_store_b32(pk(sv[0], sv[1]), c.ro, sto, (u32)(s - 1) * 32u * 1792u, 0);
    }
    if (s == NSEG) break;
#pragma unroll
    for (int p = 0; p < 2; ++p)
#pragma unroll
      for (int kt = 0; kt < 2; ++kt) {
        Y[p][4 * kt + 0] = pk(acc[2 * p][kt][0], acc[2 * p][kt][1]);
        Y[p][4 * kt + 1] = pk(acc[2 * p + 1][kt][0], acc[2 * p + 1][kt][1]);
        Y[p][4 * kt + 2] = pk(acc[2 * p][kt][2], acc[2 * p][kt][3]);
        Y[p][4 * kt + 3] = pk(acc[2 * p + 1][kt][2], acc[2 * p + 1][kt][3]);
      }
    if (s + 1 < NSEG) {
#pragma unroll
      for (int i = 0; i < 7; ++i) *(u4*)(L + L_X + (7 * w + i) * 1024 + l * 16) = xr[i];
    }
    {
      u4 A[7];
      const u32 sm = OFF_MEMA + (u32)s * 16384u + c.parM;
      for (int sp = 0; sp < SPIN_MAX; ++sp) {
        cbar();
        u32 m = 0;
#pragma unroll
        for (int i = 0; i < 7; ++i) A[i] = ld16c(c.rw, i * 1024 + l * 16, sm);
#pragma unroll
        for (int i = 0; i < 7; ++i) m = umax3(umax3(m, A[i][0], A[i][1]), A[i][2], A[i][3]);
        if (!any_sent(m)) break;
      }
      f4 u[4] = {z, z, z, z};
#pragma unroll
      for (int kb = 0; kb < 7; ++kb)
#pragma unroll
        for (int hh = 0; hh < 4; ++hh) u[hh] = smf<0>(A[kb], W[hh][kb], u[hh], idx);
#pragma unroll
      for (int p = 0; p < 2; ++p) {
        Y[2][4 * p + 0] = pk(u[2 * p][0], u[2 * p][1]);
        Y[2][4 * p + 1] = pk(u[2 * p + 1][0], u[2 * p + 1][1]);
        Y[2][4 * p + 2] = pk(u[2 * p][2], u[2 * p][3]);
        Y[2][4 * p + 3] = pk(u[2 * p + 1][2], u[2 * p + 1][3]);
      }
    }
    if (s < NSEG - 1) {
      u4 p0, p1, p2;
      const u32 so = OFF_P + (u32)s * 32768u + c.parP;
      for (int sp = 0; sp < SPIN_MAX; ++sp) {
        cbar();
        p0 = ld16c(c.rw, po[0], so); p1 = ld16c(c.rw, po[1], so); p2 = ld16c(c.rw, po[2], so);
        if (!any_sent(umax3(umax4(p0), umax4(p1), umax4(p2)))) break;
      }
      f4 o = smf<0>(p0, Y[0], z, idx);
      o = smf<0>(p1, Y[1], o, idx);
      o = smf<0>(p2, Y[2], o, idx);
#pragma unroll
      for (int r = 0; r < 4; ++r) *(float*)(L + L_RC + wr + r * 80) = o[r];
    }
    lds_bar();
    if (s < NSEG - 1 && w == 0) {
      f2 v[8];
#pragma unroll
      for (int p = 0; p < 8; ++p) v[p] = *(f2*)(L + rdc + p * 640);
      f2 sv = ((v[0] + v[1]) + (v[2] + v[3])) + ((v[4] + v[5]) + (v[6] + v[7]));
      u32 o = nosent(pk(sv[0], sv[1]));
      st4c(c.rw, stA, OFF_MEMA + (u32)(s + 1) * 16384u + c.parM, o);
      st4c(c.rw, stN, OFF_MEMN + (u32)(s + 1) * 7168u, o);
    }
  }
}

DEVI void role_X(const Ctx& c, int h, int par) {
  const int l = c.l, w = c.w, n = c.n, q = c.q;
  char* L = c.lds;
  constexpr int L_W6 = 0, L_X = 57344, L_SC = L_X + 28672;
  u4 W[6][14];
  {
    u4 t6[14];
#pragma unroll
    for (int ks = 0; ks < 14; ++ks) t6[ks] = ld16(c.rw, l * 16, OFF_WA + (u32)(((h * 4 + w) * 7 + 6) * 14 + ks) * 1024u);
#pragma unroll
    for (int tt = 0; tt < 6; ++tt)
#pragma unroll
      for (int ks = 0; ks < 14; ++ks) W[tt][ks] = ld16(c.rw, l * 16, OFF_WA + (u32)(((h * 4 + w) * 7 + tt) * 14 + ks) * 1024u);
#pragma unroll
    for (int ks = 0; ks < 14; ++ks) *(u4*)(L + L_W6 + (w * 14 + ks) * 1024 + l * 16) = t6[ks];
  }
  int kc[4];
  kc[0] = 112 * w + 16 * q; kc[1] = kc[0] + 8; kc[2] = 112 * w + 64 + 8 * q; kc[3] = 112 * w + 96 + 8 * (q >> 1);
  u32 ko[4], mo[4];
#pragma unroll
  for (int i = 0; i < 4; ++i) { ko[i] = L_X + ((kc[i] >> 5) * 64 + ((kc[i] >> 3) & 3) * 16 + n) * 16; mo[i] = (n & 7) * 896 + kc[i] * 2; }
  const f4 z = {0, 0, 0, 0};
  const u32 wsc = L_SC + (w * 32 + n) * 208;
  const int row = 8 * w + (l >> 3), sl = l & 7;
  const u32 rsc = L_SC + row * 208;
  for (int s = par; s < NSEG; s += 2) {
#pragma unroll
    for (int tt = 0; tt < 6; ++tt)
#pragma unroll
      for (int ks = 0; ks < 14; ++ks) { if (tt * 14 + ks < 54) asm volatile("" : "+a"(W[tt][ks])); else asm volatile("" : "+v"(W[tt][ks])); }
    {
      u4 xr[7];
#pragma unroll
      for (int i = 0; i < 7; ++i) { int f = 7 * w + i, mt = f >= 14, ks = f - 14 * mt; xr[i] = ld16(c.rx, (u32)(16 * mt + n) * 896u + ks * 64 + q * 16, (u32)s * 32u * 896u); }
#pragma unroll
      for (int i = 0; i < 7; ++i) *(u4*)(L + L_X + (7 * w + i) * 1024 + l * 16) = xr[i];
    }
    lds_bar();
    u4 T[2][4];
#pragma unroll
    for (int mt = 0; mt < 2; ++mt) {
      f4 acc[7];
#pragma unroll
      for (int tt = 0; tt < 7; ++tt) acc[tt] = z;
#pragma unroll
      for (int ks = 0; ks < 14; ++ks) {
        u4 b = *(u4*)(L + L_X + (14 * mt + ks) * 1024 + l * 16);
        u4 w6 = *(u4*)(L + L_W6 + (w * 14 + ks) * 1024 + l * 16);
#pragma unroll
        for (int tt = 0; tt < 6; ++tt) acc[tt] = mfma(W[tt][ks], b, acc[tt]);
        acc[6] = mfma(w6, b, acc[6]);
        if (ks & 1) __builtin_amdgcn_sched_barrier(0);
      }
#pragma unroll
      for (int p = 0; p < 3; ++p)
        T[mt][p] = (u4){pk(acc[2 * p][0], acc[2 * p][1]), pk(acc[2 * p + 1][0], acc[2 * p + 1][1]), pk(acc[2 * p][2], acc[2 * p][3]), pk(acc[2 * p + 1][2], acc[2 * p + 1][3])};
      u32 zl = pk(acc[6][0], acc[6][1]), zh = pk(acc[6][2], acc[6][3]);
      T[mt][3] = (q & 1) ? (u4){0, zl, 0, zh} : (u4){zl, 0, zh, 0};
    }
    f4 s00 = z, s01 = z, s11 = z;
#pragma unroll
    for (int i = 0; i < 4; ++i) {
      u4 k0 = *(u4*)(L + ko[i]), k1 = *(u4*)(L + ko[i] + 14 * 1024);
      s00 = mfma(k0, T[0][i], s00); s01 = mfma(k0, T[1][i], s01); s11 = mfma(k1, T[1][i], s11);
    }
    *(f4*)(L + wsc + (8 + 4 * q) * 4) = s00;
    *(f4*)(L + wsc + 16 * 208 + (8 + 4 * q) * 4) = s01;
    *(f4*)(L + wsc + 16 * 208 + (24 + 4 * q) * 4) = s11;
    u4 M[4];
    for (int sp = 0; sp < SPIN_MAX; ++sp) {
      cbar();
      u32 m = 0;
#pragma unroll
      for (int i = 0; i < 4; ++i) M[i] = ld16c(c.rw, mo[i], OFF_MEMN + (u32)s * 7168u);
#pragma unroll
      for (int i = 0; i < 4; ++i) m = umax3(umax3(m, M[i][0], M[i][1]), M[i][2], M[i][3]);
      if (!any_sent(m)) break;
      __builtin_amdgcn_s_sleep(4);
    }
    f4 m0 = z, m1 = z;
#pragma unroll
    for (int i = 0; i < 4; ++i) { m0 = mfma(M[i], T[0][i], m0); m1 = mfma(M[i], T[1][i], m1); }
    const u32 pm = (q < 2 ? 4 * q : 32 + 4 * q) * 4;
    *(f4*)(L + wsc + pm) = m0;
    *(f4*)(L + wsc + 16 * 208 + pm) = m1;
    lds_bar();
    f4 sx = z; float sm = 0;
#pragma unroll
    for (int p = 0; p < 4; ++p) { sx += *(f4*)(L + rsc + p * 6656 + 32 + sl * 16); sm += *(float*)(L + rsc + p * 6656 + sl * 4); }
    const float NEG = -1.0e9f * 1.4426950408889634f;
#pragma unroll
    for (int r = 0; r < 4; ++r) sx[r] = (4 * sl + r <= row) ? sx[r] : NEG;
    float m = fmaxf(fmaxf(fmaxf(sx[0], sx[1]), fmaxf(sx[2], sx[3])), sm);
    m = fmaxf(m, dppf<0xB1>(m)); m = fmaxf(m, dppf<0x4E>(m)); m = fmaxf(m, dppf<0x141>(m));
    float e[4], em = __builtin_amdgcn_exp2f(sm - m);
#pragma unroll
    for (int r = 0; r < 4; ++r) e[r] = __builtin_amdgcn_exp2f(sx[r] - m);
    float t = (e[0] + e[1]) + (e[2] + e[3]) + em;
    t += dppf<0xB1>(t); t += dppf<0x4E>(t); t += dppf<0x141>(t);
    float inv = __builtin_amdgcn_rcpf(t);
    em *= inv;
    float en = dppf<0xB1>(em);
    const u32 so = OFF_PX + (u32)s * 40960u + (u32)h * 2560u;
    st8c(c.rw, row * 80 + 16 + sl * 8, so, (u2){nosent(pk(e[0] * inv, e[1] * inv)), nosent(pk(e[2] * inv, e[3] * inv))});
    st4c(c.rw, (sl & 1) ? 0x80000000u : (u32)(row * 80 + sl * 2), so, nosent(pk(em, en)));
  }
}

DEVI float phi(float t) { return (t > 0.f ? t + 1.f : __expf(t)) + 1e-6f; }

DEVI void role_A(const Ctx& c, rsrc_t ra, int cu) {
  const int l = c.l, w = c.w, n = c.n, q = c.q;
  char* L = c.lds;
  constexpr int L_X = 0, L_O = 57344;
  const f4 z = {0, 0, 0, 0};
  {
    u4 xr[14];
#pragma unroll
    for (int i = 0; i < 14; ++i) xr[i] = ld16(ra, (u32)(16 * w + n) * 896u + i * 64 + q * 16, (u32)cu * 64u * 896u);
#pragma unroll
    for (int i = 0; i < 14; ++i) *(u4*)(L + L_X + (w * 14 + i) * 1024 + l * 16) = xr[i];
  }
  lds_bar();
  u4 pq[4][4];
#pragma unroll
  for (int hh = 0; hh < 4; ++hh) {
    const int h = 4 * w + hh;
    f4 aq[4][2], ak[4][2], av[4][2];
#pragma unroll
    for (int mt = 0; mt < 4; ++mt)
#pragma unroll
      for (int ct = 0; ct < 2; ++ct) aq[mt][ct] = ak[mt][ct] = av[mt][ct] = z;
#pragma unroll 2
    for (int ks = 0; ks < 14; ++ks) {
      u4 wf[3][2];
#pragma unroll
      for (int m3 = 0; m3 < 3; ++m3)
#pragma unroll
        for (int ct = 0; ct < 2; ++ct) wf[m3][ct] = ld16(c.rw, l * 16, OFF_WQKV + (u32)(((m3 * 16 + h) * 2 + ct) * 14 + ks) * 1024u);
#pragma unroll
      for (int mt = 0; mt < 4; ++mt) {
        u4 x = *(u4*)(L + L_X + (mt * 14 + ks) * 1024 + l * 16);
#pragma unroll
        for (int ct = 0; ct < 2; ++ct) {
          aq[mt][ct] = mfma(wf[0][ct], x, aq[mt][ct]);
          ak[mt][ct] = mfma(x, wf[1][ct], ak[mt][ct]);
          av[mt][ct] = mfma(x, wf[2][ct], av[mt][ct]);
        }
      }
    }
    u4 fk[2][2], fv[2][2];
#pragma unroll
    for (int ct = 0; ct < 2; ++ct)
#pragma unroll
      for (int ts = 0; ts < 2; ++ts) {
        f4 a = ak[2 * ts][ct], b = ak[2 * ts + 1][ct];
        fk[ct][ts] = (u4){pk(phi(a[0]), phi(a[1])), pk(phi(a[2]), phi(a[3])), pk(phi(b[0]), phi(b[1])), pk(phi(b[2]), phi(b[3]))};
        a = av[2 * ts][ct]; b = av[2 * ts + 1][ct];
        fv[ct][ts] = (u4){pk(a[0], a[1]), pk(a[2], a[3]), pk(b[0], b[1]), pk(b[2], b[3])};
        if (ct == 1 && n == 12) fv[ct][ts] = (u4){0x3F803F80u, 0x3F803F80u, 0x3F803F80u, 0x3F803F80u};
      }
#pragma unroll
    for (int et = 0; et < 2; ++et)
#pragma unroll
      for (int dt = 0; dt < 2; ++dt) {
        f4 kv = mfma(fk[dt][0], fv[et][0], z);
        kv = mfma(fk[dt][1], fv[et][1], kv);
        st16c(c.rw, l * 16, OFF_KVP + (u32)(((cu * 16 + h) * 2 + et) * 2 + dt) * 1024u, __builtin_bit_cast(u4, kv));
      }
#pragma unroll
    for (int mt = 0; mt < 4; ++mt) {
      f4 a = aq[mt][0], b = aq[mt][1];
      u4 t = {pk(phi(a[0]), phi(a[1])), pk(phi(a[2]), phi(a[3])), pk(phi(b[0]), phi(b[1])), pk(phi(b[2]), phi(b[3]))};
      if (q == 3) { t[2] = 0; t[3] = 0; }
      pq[hh][mt] = t;
    }
  }
  asm volatile("s_waitcnt vmcnt(0)\n\ts_barrier" ::: "memory");
  if (w == 0) st4c(c.rw, l == 0 ? (u32)cu * 128u : 0x80000000u, OFF_FLAG, 1u);
  for (int sp = 0; sp < SPIN_MAX; ++sp) {
    cbar();
    u32 f = __builtin_amdgcn_raw_buffer_load_b32(c.rw, (l & 31) * 128, OFF_FLAG, SC1);
    if (!any_sent(f)) break;
    __builtin_amdgcn_s_sleep(8);
  }
  cbar();
  {
    f4 s0 = z, s1 = z;
#pragma unroll
    for (int i = 0; i < 8; ++i) {
      u32 so = OFF_KVP + (u32)((8 * w + i) * 32 + cu) * 2048u;
      s0 += __builtin_bit_cast(f4, ld16c(c.rw, l * 16, so));
      s1 += __builtin_bit_cast(f4, ld16c(c.rw, l * 16 + 1024, so));
    }
    *(f4*)(L + L_X + (2 * w) * 1024 + l * 16) = s0;
    *(f4*)(L + L_X + (2 * w + 1) * 1024 + l * 16) = s1;
    lds_bar();
    if (w == 0) {
      s0 = z; s1 = z;
#pragma unroll
      for (int i = 0; i < 4; ++i) { s0 += *(f4*)(L + L_X + (2 * i) * 1024 + l * 16); s1 += *(f4*)(L + L_X + (2 * i + 1) * 1024 + l * 16); }
      st16c(c.rw, l * 16, OFF_KVF + (u32)cu * 1024u, (u4){nosent(pk(s0[0], s0[1])), nosent(pk(s0[2], s0[3])), nosent(pk(s1[0], s1[1])), nosent(pk(s1[2], s1[3]))});
    }
  }
  u4 kf[8];
  for (int sp = 0; sp < SPIN_MAX; ++sp) {
    cbar();
    u32 m = 0;
#pragma unroll
    for (int i = 0; i < 8; ++i) kf[i] = ld16c(c.rw, l * 16 + i * 1024, OFF_KVF + (u32)w * 8192u);
#pragma unroll
    for (int i = 0; i < 8; ++i) m = umax3(umax3(m, kf[i][0], kf[i][1]), kf[i][2], kf[i][3]);
    if (!any_sent(m)) break;
    __builtin_amdgcn_s_sleep(8);
  }
#pragma unroll
  for (int hh = 0; hh < 4; ++hh)
#pragma unroll
    for (int mt = 0; mt < 4; ++mt) {
      f4 a = mfma(kf[2 * hh], pq[hh][mt], z), b = mfma(kf[2 * hh + 1], pq[hh][mt], z);
      float den = __shfl(b[0], 48 + n) + 1e-6f;
      float inv = 1.0f / den;
      a *= inv; b *= inv;
      u4 t = {pk(a[0], a[1]), pk(a[2], a[3]), pk(b[0], b[1]), pk(b[2], b[3])};
      if (q == 3) { t[2] = 0; t[3] = 0; }
      *(u4*)(L + L_O + ((4 * w + hh) * 4 + mt) * 1024 + l * 16) = t;
    }
  lds_bar();
  f4 acc[7][4];
#pragma unroll
  for (int t = 0; t < 7; ++t)
#pragma unroll
    for (int mt = 0; mt < 4; ++mt) acc[t][mt] = z;
#pragma unroll 2
  for (int h = 0; h < 16; ++h) {
    u4 wf[7];
#pragma unroll
    for (int t = 0; t < 7; ++t) wf[t] = ld16(c.rw, l * 16, OFF_WO + (u32)((7 * w + t) * 16 + h) * 1024u);
#pragma unroll
    for (int mt = 0; mt < 4; ++mt) {
      u4 o = *(u4*)(L + L_O + (h * 4 + mt) * 1024 + l * 16);
#pragma unroll
      for (int t = 0; t < 7; ++t) acc[t][mt] = mfma(wf[t], o, acc[t][mt]);
    }
  }
#pragma unroll
  for (int mt = 0; mt < 4; ++mt)
#pragma unroll
    for (int t = 0; t < 7; ++t) {
      f4 a = acc[t][mt];
      __builtin_amdgcn_raw_buffer_store_b64((u2){pk(a[0], a[1]), pk(a[2], a[3])}, c.ro, (u32)(16 * mt + n) * 1792u + (7 * w + t) * 32 + q * 8, (u32)cu * 64u * 1792u, 0);
    }
}

#define PARM 0
#define PARP 0

__global__ void __launch_bounds__(256, 1)
mainq(const void* ax, const void* bx, void* out, char* ws) {
  extern __shared__ __attribute__((aligned(16))) char lds[];
  Ctx c;
  c.rw = mk(ws, WS_USED);
  c.rx = mk(bx, 2048u * 896u);
  c.ro = mk(out, 2048u * 1792u);
  c.lds = lds;
  c.l = threadIdx.x & 63; c.w = __builtin_amdgcn_readfirstlane(threadIdx.x >> 6);
  c.n = c.l & 15; c.q = c.l >> 4;
  c.parM = PARM * 8192; c.parP = PARP * 8192;
  const int xc = blockIdx.x & 7, i = blockIdx.x >> 3;
  if (xc == 0) { if (i < 28) role_V(c, i); }
  else if (xc == 1) { if (i < 16) role_S(c, i); }
  else if (xc == 2 || xc == 3) { if (i < 16) role_X(c, (xc - 2) * 8 + (i >> 1), i & 1); }
  else if (i < 8) role_A(c, mk(ax, 2048u * 896u), (xc - 4) * 8 + i);
}

extern "C" void kernel_launch(void* const* d_in, const int* in_sizes, int n_in, void* d_out, int out_size, void* d_ws, size_t ws_size, hipStream_t stream) {
  char* ws = (char*)(((uintptr_t)d_ws + MB2 - 1) & ~(uintptr_t)(MB2 - 1));
  (void)hipFuncSetAttribute((const void*)mainq, hipFuncAttributeMaxDynamicSharedMemorySize, LDS_BYTES);
  prologue<<<256, 256, 0, stream>>>(d_in[1], d_in[2], d_in[4], d_in[5], d_in[6], d_in[7], d_in[8], ws);
  mainq<<<256, 256, LDS_BYTES, stream>>>(d_in[0], d_in[3], d_out, ws);
}
```

```cpp
#include <hip/hip_runtime.h>
#include <hip/hip_bf16.h>
#include <stdint.h>

typedef unsigned u32;
typedef u32 u2 __attribute__((ext_vector_type(2)));
typedef u32 u4 __attribute__((ext_vector_type(4)));
typedef u32 u8v __attribute__((ext_vector_type(8)));
typedef float f2 __attribute__((ext_vector_type(2)));
typedef float f4 __attribute__((ext_vector_type(4)));
typedef __bf16 b2 __attribute__((ext_vector_type(2)));
typedef __bf16 b8 __attribute__((ext_vector_type(8)));
typedef __bf16 b16v __attribute__((ext_vector_type(16)));
typedef __amdgpu_buffer_rsrc_t rsrc_t;

#define DEVI __device__ __forceinline__
constexpr int SC1 = 16;
constexpr int D = 448, NSEG = 64;

#define YR 64
constexpr u32 MB2 = 2u << 20;
constexpr u32 OFF_MEMA = 0;
constexpr u32 OFF_P    = MB2;
constexpr u32 OFF_PX   = 2 * MB2;
constexpr u32 OFF_KVF  = OFF_PX + 64 * 40960;
constexpr u32 OFF_FLAG = OFF_KVF + 32768;
constexpr u32 OFF_YF   = OFF_FLAG + 4096;
constexpr u32 OFF_INIT_END = OFF_YF + 28 * 4 * 256;
constexpr u32 OFF_PW   = OFF_INIT_END;
constexpr u32 OFF_F1   = OFF_PW + 128;
constexpr u32 OFF_WA   = 4 * MB2;
constexpr u32 WSZ      = 16 * 28 * 14 * 1024;
constexpr u32 OFF_WB   = OFF_WA + WSZ;
constexpr u32 OFF_WQKV = OFF_WB + WSZ;
constexpr u32 OFF_WO   = OFF_WQKV + 1344 * 1024;
constexpr u32 OFF_KVP  = OFF_WO + 448 * 1024;
constexpr u32 OFF_Y    = 13 * MB2;
constexpr u32 OFF_DBG  = OFF_Y + YR * 28 * 16384;
static_assert(OFF_KVP + 32 * 16 * 4096 <= OFF_Y, "layout");
constexpr u32 WS_USED  = OFF_DBG + 90112;
static_assert(OFF_F1 + 49152 <= OFF_WA && OFF_INIT_END % 1024 == 0, "layout");

constexpr int PH2 = 16384;
constexpr int SPIN_MAX = 1 << 20;

DEVI rsrc_t mk(const void* p, u32 bytes) { return __builtin_amdgcn_make_buffer_rsrc((void*)p, (short)0, (int)bytes, 0x00020000); }
DEVI u4 ld16(rsrc_t r, u32 v, u32 s) { return __builtin_amdgcn_raw_buffer_load_b128(r, v, s, 0); }
DEVI u4 ld16c(rsrc_t r, u32 v, u32 s) { return __builtin_amdgcn_raw_buffer_load_b128(r, v, s, SC1); }
DEVI void st16(rsrc_t r, u32 v, u32 s, u4 x) { __builtin_amdgcn_raw_buffer_store_b128(x, r, v, s, 0); }
DEVI void st16c(rsrc_t r, u32 v, u32 s, u4 x) { __builtin_amdgcn_raw_buffer_store_b128(x, r, v, s, SC1); }
DEVI void st8c(rsrc_t r, u32 v, u32 s, u2 x) { __builtin_amdgcn_raw_buffer_store_b64(x, r, v, s, SC1); }
DEVI void st4c(rsrc_t r, u32 v, u32 s, u32 x) { __builtin_amdgcn_raw_buffer_store_b32(x, r, v, s, SC1); }
DEVI u32 pk(float a, float b) { f2 t = {a, b}; return __builtin_bit_cast(u32, __builtin_convertvector(t, b2)); }
DEVI u32 nosent(u32 x) { return x < 0xFFFFFFFEu ? x : 0xFFFFFFFEu; }
DEVI f4 mfma(u4 a, u4 b, f4 c) { return __builtin_amdgcn_mfma_f32_16x16x32_bf16(__builtin_bit_cast(b8, a), __builtin_bit_cast(b8, b), c, 0, 0, 0); }
template <int AB> DEVI f4 smf(u4 a, u8v b, f4 c, int idx) { return __builtin_amdgcn_smfmac_f32_16x16x64_bf16(__builtin_bit_cast(b8, a), __builtin_bit_cast(b16v, b), c, idx, 0, AB); }
DEVI u8v cat(u4 a, u4 b) { return __builtin_shufflevector(a, b, 0, 1, 2, 3, 4, 5, 6, 7); }
DEVI u4 lo4(u8v a) { return __builtin_shufflevector(a, a, 0, 1, 2, 3); }
DEVI u4 hi4(u8v a) { return __builtin_shufflevector(a, a, 4, 5, 6, 7); }
DEVI u32 umax3(u32 a, u32 b, u32 c) { u32 m = a > b ? a : b; return m > c ? m : c; }
DEVI u32 umax4(u4 a) { u32 m = umax3(a[0], a[1], a[2]); return m > a[3] ? m : a[3]; }
DEVI bool any_sent(u32 m) { return __builtin_amdgcn_ballot_w64(m == 0xFFFFFFFFu) != 0; }
DEVI int lane_id() { int x; asm volatile("v_mbcnt_lo_u32_b32 %0, -1, 0\n\tv_mbcnt_hi_u32_b32 %0, -1, %0" : "=v"(x)); return x & 63; }
DEVI void cbar() { asm volatile("" ::: "memory"); }
DEVI void lds_bar() { asm volatile("s_waitcnt lgkmcnt(0)\n\ts_barrier" ::: "memory"); }
template <int C> DEVI float dppf(float x) { return __builtin_bit_cast(float, __builtin_amdgcn_update_dpp(0, __builtin_bit_cast(int, x), C, 0xf, 0xf, true)); }

#define RTP 0
#define RTO(k)
#define RTT(k)
DEVI int colmap(int w, int tt, int m) {
  int q = m >> 2, r = m & 3, b = 112 * w + 4 * (r >> 1) + (r & 1);
  if (tt < 4) return b + 16 * q + 8 * (tt >> 1) + 2 * (tt & 1);
  if (tt < 6) return b + 64 + 8 * q + 2 * (tt - 4);
  return b + 96 + 8 * (q >> 1) + 2 * (q & 1);
}

constexpr int LDS_BYTES = 160 * 1024;

struct Ctx {
  rsrc_t rw, rx, ro;
  char* lds;
  int w;
  u32 d0, d1, d2;
  int up, dn;
  int isS, isV, isO, isX;
  u32 xt, ot;
  u32 e0, e1;
};

#define MIDST 1
#define TWOP 0
#define HSC ""
#define ODEL 4
#define XD0 0
#define XD1 0
#define ADEL 20
#define NDIV 1
#define TOUCH 0
#define CODEPF 0
#define OSC 0
#define ASC 0
#define OWB 0
#define AWB 0
#define FPOLL 4
#define WLD SC1
DEVI u4 ld16w(rsrc_t r, u32 v, u32 s) { return __builtin_amdgcn_raw_buffer_load_b128(r, v, s, WLD); }

DEVI u32 uni(u32 x) { return __builtin_amdgcn_readfirstlane(x); }
DEVI u32 lds_addr(char* p) { return (u32)(uintptr_t)(__attribute__((address_space(3))) char*)p; }
DEVI void dma16(rsrc_t r, u32 ldsa, u32 v, u32 so) {
  asm volatile("s_mov_b32 m0, %0\n\ts_nop 0\n\tbuffer_load_dwordx4 %1, %2, %3 offen lds" :: "s"(ldsa), "v"(v), "s"(r), "s"(so) : "memory");
}
DEVI void dma16i(rsrc_t r, u32 ldsa, int lo, u32 v, int so) {
  u32 t;
  asm volatile("s_add_u32 m0, %1, %2\n\ts_mov_b32 %0, %5\n\tbuffer_load_dwordx4 %3, %4, %0 offen lds" : "=&s"(t) : "s"(ldsa), "n"(lo), "v"(v), "s"(r), "n"(so) : "memory", "scc");
}
__host__ __device__ __forceinline__ int roleof(int place, int bid, int& a0, int& a1) {
  const int xc = bid & 7, i = bid >> 3;
  int role = -1; a0 = 0, a1 = 0;
  if (xc == 0) { if (i < 28) role = 0, a0 = i; else a0 = i; }
  else if (xc == 1) { if (i < 16) role = 1, a0 = i; else if (i == 31) role = 5; else a0 = i + 16; }
  else if (xc < 4) { if (i < 24) role = 3, a0 = (xc - 2) * 8 + i / 3, a1 = i % 3; else a0 = xc * 8 + i - 40; }
  else if (i < 21) role = 2, a0 = (xc - 4) * 7 + i / 3, a1 = i % 3;
  else if (i < 29) role = 4, a0 = (xc - 4) * 8 + i - 21;
  else a0 = xc * 3 + i - 25;
  return role;
}

constexpr int L_CST = 50176;
DEVI void consts_S(const Ctx& c, int h, u32* k) {
  const int l = lane_id(), w = c.w, n = l & 15, q = l >> 4;
  char* L = c.lds;
  const u32 LB = lds_addr(L);
  constexpr int L_X = 0, L_MEM = 28672, L_SC = L_MEM + 8192;
  k[0] = ((n & 1) ? 0x00EEEEEEu : 0x00444444u) | ((q & 1) ? 0xEE000000u : 0x44000000u);
  k[1] = 2 * w * 1024 + l * 16;
  k[2] = (w == 3 ? 0 : 2 * w + 1) * 1024 + l * 16;
  k[3] = w == 0 ? (q == 3 ? 5184 : ((l >> 3) & 1) * PH2 + 5120 + (l & 7) * 128 + 8 - (q & 1) * 4) : 0x80000000u;
  const int pa = w == 0 ? 1 : (w == 3 ? 1 : 0), pb = w == 2 ? 1 : 2, sg = w == 3 ? 1 : 6;
  k[4] = LB + L_MEM + pa * 2048 + l * 16;
  k[5] = LB + L_MEM + pb * 2048 + l * 16;
  k[6] = LB + L_MEM + sg * 1024 + l * 16;
  k[7] = (u32)(8 * w + (l >> 3)) * 896u + (((l & 7) ^ (4 * (w & 1) + (l >> 4))) * 16);
  int kc[4];
  kc[0] = 112 * w + 8 * q; kc[1] = kc[0] + 32;
  kc[2] = 112 * w + ((q & 1) ? 96 : 64 + 4 * q);
  kc[3] = 112 * w + ((q & 1) ? 104 : 80 + 4 * q);
#pragma unroll
  for (int i = 0; i < 4; ++i) {
    int col = kc[i];
    k[8 + i] = LB + L_X + (n >> 3) * 7168 + (col >> 6) * 1024 + (n & 7) * 128 + ((((col >> 3) & 7) ^ (n >> 1)) * 16);
    k[12 + i] = LB + L_MEM + (col >> 6) * 1024 + ((col >> 4) & 3) * 256 + (n & 7) * 32 + ((col >> 3) & 1) * 8;
  }
  const u32 wsc = LB + L_SC + (w * 328 + 2 * q * 40) * 4;
  k[16] = wsc + n * 4;
  k[17] = n < 8 ? k[16] : LB + L_SC + 5248 + l * 4;
  const int slot = l & 31, row = 2 * w + (l >> 5);
  k[18] = LB + L_SC + (slot < 20 ? row * 40 + 2 * slot : 320) * 4;
  const int oc = (slot - 4) >> 2;
  k[19] = slot < 4 ? (u32)(4096 + ((h & 7) >> 1) * 256 + w * 64 + (h & 1) * 32) : (u32)((h & 7) * 512 + w * 128 + (oc & 1) * 64 + (oc >> 1) * 32);
  k[19] = slot < 20 ? k[19] + (h >> 3) * PH2 + (l >> 5) * 16 + (slot & 3) * 4 : 0x80000000u;
#pragma unroll
  for (int d = 0; d < 4; ++d) {
    u32 v = 0;
#pragma unroll
    for (int e = 0; e < 2; ++e) { int j = 2 * d + e; if ((n >> 1) == 2 * q + ((j & 3) >> 1) && (n & 1) == (j >> 2)) v |= 0x3F80u << (16 * e); }
    k[20 + d] = v;
  }
}
DEVI void consts_V(const Ctx& c, int j, u32* k) {
  const int l = lane_id(), w = c.w, n = l & 15, q = l >> 4;
  char* L = c.lds;
  const u32 LB = lds_addr(L);
  constexpr int L_M = 0, L_U = 8192;
  k[0] = (n & 1) ? 0xEEEEu : 0x4444u;
  k[1] = w == 0 ? l * 16 : LB + L_M + (w == 3 ? 6144 : 7168) + l * 16;
  k[2] = 2 * (w - 1) * 1024 + l * 16;
  k[3] = LB + L_M + ((2 * w + 3) % 7) * 1024 + l * 16;
  k[4] = LB + L_M + k[2];
  k[5] = LB + L_M + 2 * w * 1024 + l * 16;
  k[6] = LB + L_U + w * 1024 + ((q >> 1) * 16 + n) * 16 + (q & 1) * 8;
  k[7] = (j >> 2) * 1024 + (j & 3) * 256 + l * 4;
  k[8] = (q >> 1) * 512 + (n >> 2) * 128 + (q & 1) * 64 + (n & 3) * 16;
  k[9] = w == 1 ? ((l >> 3) & 1) * PH2 + 5120 + (l & 7) * 128 + (q & 1) * 4 : 0x80000000u;
  k[10] = w == 0 ? (l & 3) * 256 : 0x80000000u;
  k[11] = (n >> 3) ? 0x03020706u : 0x05040100u;
  k[12] = (n & 2) ? 0xEEEEu : 0x4444u;
  k[13] = w == 0 ? LB + L_U + l * 16 : LB + L_M + ((2 * w + 1) % 7) * 1024 + l * 16;
  k[14] = 0;
  k[15] = q == (j & 3) && (w == 3 || (j >> 2) != 2 * w) ? (j >> 2) - 2 * (w - 1) : -1;
  if (w == 0) k[3] = LB + L_M + k[7];
}
#define ASM_P1 \
  "v_xor_b32 v236, 0, %[oa]\n" \
  "v_xor_b32 v237, 32, %[oa]\n" \
  "v_xor_b32 v238, 64, %[oa]\n" \
  "v_xor_b32 v239, 96, %[oa]\n" \
  "v_xor_b32 v240, 128, %[oa]\n" \
  "v_xor_b32 v241, 160, %[oa]\n" \
  "v_xor_b32 v242, 192, %[oa]\n" \
  "v_xor_b32 v243, 224, %[oa]\n" \
  "s_add_u32 m0, %[la], 0\n" \
  "s_mov_b32 s40, 0\n" \
  "buffer_load_dwordx4 v236, %[ra], s40 offen lds\n" \
  "s_add_u32 m0, %[la], 1024\n" \
  "s_mov_b32 s40, 16384\n" \
  "buffer_load_dwordx4 v237, %[ra], s40 offen lds\n" \
  "s_add_u32 m0, %[la], 2048\n" \
  "s_mov_b32 s40, 65536\n" \
  "buffer_load_dwordx4 v238, %[ra], s40 offen lds\n" \
  "s_add_u32 m0, %[la], 3072\n" \
  "s_mov_b32 s40, 81920\n" \
  "buffer_load_dwordx4 v239, %[ra], s40 offen lds\n" \
  "s_add_u32 m0, %[la], 4096\n" \
  "s_mov_b32 s40, 131072\n" \
  "buffer_load_dwordx4 v240, %[ra], s40 offen lds\n" \
  "s_add_u32 m0, %[la], 5120\n" \
  "s_mov_b32 s40, 147456\n" \
  "buffer_load_dwordx4 v241, %[ra], s40 offen lds\n" \
  "s_add_u32 m0, %[la], 6144\n" \
  "s_mov_b32 s40, 196608\n" \
  "buffer_load_dwordx4 v242, %[ra], s40 offen lds\n" \
  "s_add_u32 m0, %[la], 7168\n" \
  "s_mov_b32 s40, 212992\n" \
  "buffer_load_dwordx4 v243, %[ra], s40 offen lds\n" \
  "s_cmp_eq_u32 %[wv], 3\n" \
  "s_cbranch_scc1 49f\n" \
  "s_add_u32 m0, %[la], 8192\n" \
  "s_mov_b32 s40, 32768\n" \
  "buffer_load_dwordx4 v236, %[ra], s40 offen lds\n" \
  "s_add_u32 m0, %[la], 9216\n" \
  "s_mov_b32 s40, 49152\n" \
  "buffer_load_dwordx4 v237, %[ra], s40 offen lds\n" \
  "s_add_u32 m0, %[la], 10240\n" \
  "s_mov_b32 s40, 98304\n" \
  "buffer_load_dwordx4 v238, %[ra], s40 offen lds\n" \
  "s_add_u32 m0, %[la], 11264\n" \
  "s_mov_b32 s40, 114688\n" \
  "buffer_load_dwordx4 v239, %[ra], s40 offen lds\n" \
  "s_add_u32 m0, %[la], 12288\n" \
  "s_mov_b32 s40, 163840\n" \
  "buffer_load_dwordx4 v240, %[ra], s40 offen lds\n" \
  "s_add_u32 m0, %[la], 13312\n" \
  "s_mov_b32 s40, 180224\n" \
  "buffer_load_dwordx4 v241, %[ra], s40 offen lds\n" \
  "s_add_u32 m0, %[la], 14336\n" \
  "s_mov_b32 s40, 229376\n" \
  "buffer_load_dwordx4 v242, %[ra], s40 offen lds\n" \
  "s_add_u32 m0, %[la], 15360\n" \
  "s_mov_b32 s40, 245760\n" \
  "buffer_load_dwordx4 v243, %[ra], s40 offen lds\n" \
  "49:\n"
#define ASM_P \
  "v_xor_b32 v236, 0, %[oa]\n" \
  "v_xor_b32 v237, 32, %[oa]\n" \
  "v_xor_b32 v238, 64, %[oa]\n" \
  "v_xor_b32 v239, 96, %[oa]\n" \
  "v_xor_b32 v240, 128, %[oa]\n" \
  "v_xor_b32 v241, 160, %[oa]\n" \
  "v_xor_b32 v242, 192, %[oa]\n" \
  "v_xor_b32 v243, 224, %[oa]\n" \
  "v_add_u32 v244, %[la], %[ro0]\n" \
  "v_add_u32 v245, %[la], %[ro1]\n" \
  "v_add_u32 v246, %[la], %[ro2]\n" \
  "v_add_u32 v247, %[la], %[ro3]\n" \
  "s_cmp_eq_u32 %[wv], 3\n" \
  "s_cbranch_scc1 50f\n" \
  "s_add_u32 m0, %[la], 16384\n" \
  "s_mov_b32 s40, 1048576\n" \
  "buffer_load_dwordx4 v236, %[ra], s40 offen lds\n" \
  "s_add_u32 m0, %[la], 17408\n" \
  "s_mov_b32 s40, 1064960\n" \
  "buffer_load_dwordx4 v237, %[ra], s40 offen lds\n" \
  "s_add_u32 m0, %[la], 18432\n" \
  "s_mov_b32 s40, 1114112\n" \
  "buffer_load_dwordx4 v238, %[ra], s40 offen lds\n" \
  "s_add_u32 m0, %[la], 19456\n" \
  "s_mov_b32 s40, 1130496\n" \
  "buffer_load_dwordx4 v239, %[ra], s40 offen lds\n" \
  "s_add_u32 m0, %[la], 20480\n" \
  "s_mov_b32 s40, 1179648\n" \
  "buffer_load_dwordx4 v240, %[ra], s40 offen lds\n" \
  "s_add_u32 m0, %[la], 21504\n" \
  "s_mov_b32 s40, 1196032\n" \
  "buffer_load_dwordx4 v241, %[ra], s40 offen lds\n" \
  "s_add_u32 m0, %[la], 22528\n" \
  "s_mov_b32 s40, 1245184\n" \
  "buffer_load_dwordx4 v242, %[ra], s40 offen lds\n" \
  "s_add_u32 m0, %[la], 23552\n" \
  "s_mov_b32 s40, 1261568\n" \
  "buffer_load_dwordx4 v243, %[ra], s40 offen lds\n" \
  "s_add_u32 m0, %[la], 24576\n" \
  "s_mov_b32 s40, 1081344\n" \
  "buffer_load_dwordx4 v236, %[ra], s40 offen lds\n" \
  "s_add_u32 m0, %[la], 25600\n" \
  "s_mov_b32 s40, 1097728\n" \
  "buffer_load_dwordx4 v237, %[ra], s40 offen lds\n" \
  "s_add_u32 m0, %[la], 26624\n" \
  "s_mov_b32 s40, 1146880\n" \
  "buffer_load_dwordx4 v238, %[ra], s40 offen lds\n" \
  "s_add_u32 m0, %[la], 27648\n" \
  "s_mov_b32 s40, 1163264\n" \
  "buffer_load_dwordx4 v239, %[ra], s40 offen lds\n" \
  "s_add_u32 m0, %[la], 28672\n" \
  "s_mov_b32 s40, 1212416\n" \
  "buffer_load_dwordx4 v240, %[ra], s40 offen lds\n" \
  "s_add_u32 m0, %[la], 29696\n" \
  "s_mov_b32 s40, 1228800\n" \
  "buffer_load_dwordx4 v241, %[ra], s40 offen lds\n" \
  "s_add_u32 m0, %[la], 30720\n" \
  "s_mov_b32 s40, 1277952\n" \
  "buffer_load_dwordx4 v242, %[ra], s40 offen lds\n" \
  "s_add_u32 m0, %[la], 31744\n" \
  "s_mov_b32 s40, 1294336\n" \
  "buffer_load_dwordx4 v243, %[ra], s40 offen lds\n" \
  "s_movk_i32 s41, 0x1000\n" \
  "s_waitcnt vmcnt(16)\n" \
  "ds_read_b128 v[32:35], v244 offset:0\n" \
  "ds_read_b128 v[36:39], v245 offset:0\n" \
  "ds_read_b128 v[40:43], v246 offset:0\n" \
  "ds_read_b128 v[44:47], v247 offset:0\n" \
  "ds_read_b128 v[48:51], v244 offset:256\n" \
  "ds_read_b128 v[52:55], v245 offset:256\n" \
  "ds_read_b128 v[56:59], v246 offset:256\n" \
  "ds_read_b128 v[60:63], v247 offset:256\n" \
  "ds_read_b128 v[64:67], v244 offset:8192\n" \
  "ds_read_b128 v[68:71], v245 offset:8192\n" \
  "ds_read_b128 v[72:75], v246 offset:8192\n" \
  "ds_read_b128 v[76:79], v247 offset:8192\n" \
  "ds_read_b128 v[80:83], v244 offset:8448\n" \
  "ds_read_b128 v[84:87], v245 offset:8448\n" \
  "ds_read_b128 v[88:91], v246 offset:8448\n" \
  "ds_read_b128 v[92:95], v247 offset:8448\n" \
  "s_barrier\n" \
  "s_waitcnt vmcnt(8)\n" \
  "ds_read_b128 v[96:99], v244 offset:16384\n" \
  "ds_read_b128 v[100:103], v245 offset:16384\n" \
  "ds_read_b128 v[104:107], v246 offset:16384\n" \
  "ds_read_b128 v[108:111], v247 offset:16384\n" \
  "ds_read_b128 v[112:115], v244 offset:16640\n" \
  "ds_read_b128 v[116:119], v245 offset:16640\n" \
  "ds_read_b128 v[120:123], v246 offset:16640\n" \
  "ds_read_b128 v[124:127], v247 offset:16640\n" \
  "s_waitcnt vmcnt(0)\n" \
  "ds_read_b128 v[128:131], v244 offset:24576\n" \
  "ds_read_b128 v[132:135], v245 offset:24576\n" \
  "ds_read_b128 v[136:139], v246 offset:24576\n" \
  "ds_read_b128 v[140:143], v247 offset:24576\n" \
  "ds_read_b128 v[144:147], v244 offset:24832\n" \
  "ds_read_b128 v[148:151], v245 offset:24832\n" \
  "ds_read_b128 v[152:155], v246 offset:24832\n" \
  "ds_read_b128 v[156:159], v247 offset:24832\n" \
  "s_branch 51f\n" \
  "50:\n" \
  "buffer_load_dwordx4 v[64:67], %[od], %[ra], 0 offen offset:0\n" \
  "buffer_load_dwordx4 v[68:71], %[od], %[ra], 0 offen offset:64\n" \
  "buffer_load_dwordx4 v[72:75], %[od], %[ra], 0 offen offset:128\n" \
  "buffer_load_dwordx4 v[76:79], %[od], %[ra], 0 offen offset:192\n" \
  "buffer_load_dwordx4 v[80:83], %[od], %[ra], 0 offen offset:256\n" \
  "buffer_load_dwordx4 v[84:87], %[od], %[ra], 0 offen offset:320\n" \
  "buffer_load_dwordx4 v[88:91], %[od], %[ra], 0 offen offset:384\n" \
  "buffer_load_dwordx4 v[92:95], %[od], %[ra], 0 offen offset:448\n" \
  "s_brev_b32 s41, 1\n" \
  "v_mov_b64 v[96:97], 0\n" \
  "v_mov_b64 v[98:99], 0\n" \
  "v_mov_b64 v[100:101], 0\n" \
  "v_mov_b64 v[102:103], 0\n" \
  "v_mov_b64 v[104:105], 0\n" \
  "v_mov_b64 v[106:107], 0\n" \
  "v_mov_b64 v[108:109], 0\n" \
  "v_mov_b64 v[110:111], 0\n" \
  "v_mov_b64 v[112:113], 0\n" \
  "v_mov_b64 v[114:115], 0\n" \
  "v_mov_b64 v[116:117], 0\n" \
  "v_mov_b64 v[118:119], 0\n" \
  "v_mov_b64 v[120:121], 0\n" \
  "v_mov_b64 v[122:123], 0\n" \
  "v_mov_b64 v[124:125], 0\n" \
  "v_mov_b64 v[126:127], 0\n" \
  "v_mov_b64 v[128:129], 0\n" \
  "v_mov_b64 v[130:131], 0\n" \
  "v_mov_b64 v[132:133], 0\n" \
  "v_mov_b64 v[134:135], 0\n" \
  "v_mov_b64 v[136:137], 0\n" \
  "v_mov_b64 v[138:139], 0\n" \
  "v_mov_b64 v[140:141], 0\n" \
  "v_mov_b64 v[142:143], 0\n" \
  "v_mov_b64 v[144:145], 0\n" \
  "v_mov_b64 v[146:147], 0\n" \
  "v_mov_b64 v[148:149], 0\n" \
  "v_mov_b64 v[150:151], 0\n" \
  "v_mov_b64 v[152:153], 0\n" \
  "v_mov_b64 v[154:155], 0\n" \
  "v_mov_b64 v[156:157], 0\n" \
  "v_mov_b64 v[158:159], 0\n" \
  "s_waitcnt vmcnt(8)\n" \
  "ds_read_b128 v[32:35], v244 offset:0\n" \
  "ds_read_b128 v[36:39], v245 offset:0\n" \
  "ds_read_b128 v[40:43], v246 offset:0\n" \
  "ds_read_b128 v[44:47], v247 offset:0\n" \
  "ds_read_b128 v[48:51], v244 offset:256\n" \
  "ds_read_b128 v[52:55], v245 offset:256\n" \
  "ds_read_b128 v[56:59], v246 offset:256\n" \
  "ds_read_b128 v[60:63], v247 offset:256\n" \
  "s_barrier\n" \
  "s_waitcnt vmcnt(0)\n" \
  "51:\n" \
  "buffer_load_dword v243, %[xp], %[rx], 0 offen\n" \
  "s_mov_b32 s47, 0x3db8aa3b\n" \
  "s_cmp_lg_u32 %[mat], 0\n" \
  "s_cbranch_scc1 60f\n" \
  "s_mov_b32 s46, 8192\n" \
  "s_sub_u32 s44, %[fb], 14336\n" \
  "v_add_u32 v232, %[lb], %[ro0]\n" \
  "v_add_u32 v233, %[lb], %[ro1]\n" \
  "v_add_u32 v234, %[lb], %[ro2]\n" \
  "v_add_u32 v235, %[lb], %[ro3]\n" \
  "ds_read_b128 v[160:163], v232 offset:0\n" \
  "ds_read_b128 v[164:167], v233 offset:0\n" \
  "ds_read_b128 v[168:171], v234 offset:0\n" \
  "ds_read_b128 v[172:175], v235 offset:0\n" \
  "ds_read_b128 v[176:179], v232 offset:256\n" \
  "ds_read_b128 v[180:183], v233 offset:256\n" \
  "ds_read_b128 v[184:187], v234 offset:256\n" \
  "ds_read_b128 v[188:191], v235 offset:256\n" \
  "v_add_u32 v232, s46, v232\n" \
  "v_add_u32 v233, s46, v233\n" \
  "v_bfrev_b32 v238, 1\n" \
  "v_bfrev_b32 v239, 1\n" \
  "s_mov_b32 s42, 6\n" \
  "s_waitcnt lgkmcnt(6)\n" \
  "52:\n" \
  "v_mfma_f32_16x16x32_bf16 v[192:195], v[32:35], v[160:163], 0\n" \
  "v_add_u32 v234, s46, v234\n" \
  "v_add_u32 v235, s46, v235\n" \
  "v_mfma_f32_16x16x32_bf16 v[196:199], v[64:67], v[160:163], 0\n" \
  "v_mfma_f32_16x16x32_bf16 v[200:203], v[96:99], v[160:163], 0\n" \
  "v_mfma_f32_16x16x32_bf16 v[204:207], v[128:131], v[160:163], 0\n" \
  "ds_read_b128 v[160:163], v232 offset:0\n" \
  "v_mul_f32 v208, s47, v208\n" \
  "v_mfma_f32_16x16x32_bf16 v[192:195], v[36:39], v[164:167], v[192:195]\n" \
  "v_mul_f32 v209, s47, v209\n" \
  "v_mfma_f32_16x16x32_bf16 v[196:199], v[68:71], v[164:167], v[196:199]\n" \
  "v_mul_f32 v210, s47, v210\n" \
  "v_mfma_f32_16x16x32_bf16 v[200:203], v[100:103], v[164:167], v[200:203]\n" \
  "v_mul_f32 v211, s47, v211\n" \
  "v_mfma_f32_16x16x32_bf16 v[204:207], v[132:135], v[164:167], v[204:207]\n" \
  "ds_read_b128 v[164:167], v233 offset:0\n" \
  "s_waitcnt lgkmcnt(6)\n" \
  "v_mul_f32 v212, s47, v212\n" \
  "v_mfma_f32_16x16x32_bf16 v[192:195], v[40:43], v[168:171], v[192:195]\n" \
  "v_mul_f32 v213, s47, v213\n" \
  "v_mfma_f32_16x16x32_bf16 v[196:199], v[72:75], v[168:171], v[196:199]\n" \
  "v_mul_f32 v214, s47, v214\n" \
  "v_mfma_f32_16x16x32_bf16 v[200:203], v[104:107], v[168:171], v[200:203]\n" \
  "v_mul_f32 v215, s47, v215\n" \
  "v_mfma_f32_16x16x32_bf16 v[204:207], v[136:139], v[168:171], v[204:207]\n" \
  "ds_read_b128 v[168:171], v234 offset:0\n" \
  "v_cvt_pk_bf16_f32 v224, v208, v209\n" \
  "v_mfma_f32_16x16x32_bf16 v[192:195], v[44:47], v[172:175], v[192:195]\n" \
  "v_cvt_pk_bf16_f32 v225, v210, v211\n" \
  "v_mfma_f32_16x16x32_bf16 v[196:199], v[76:79], v[172:175], v[196:199]\n" \
  "v_cvt_pk_bf16_f32 v226, v212, v213\n" \
  "v_mfma_f32_16x16x32_bf16 v[200:203], v[108:111], v[172:175], v[200:203]\n" \
  "v_cvt_pk_bf16_f32 v227, v214, v215\n" \
  "v_mfma_f32_16x16x32_bf16 v[204:207], v[140:143], v[172:175], v[204:207]\n" \
  "ds_read_b128 v[172:175], v235 offset:0\n" \
  "s_waitcnt lgkmcnt(6)\n" \
  "buffer_store_dwordx4 v[224:227], v238, %[rw], s44 offen sc1\n" \
  "v_mul_f32 v216, s47, v216\n" \
  "v_mfma_f32_16x16x32_bf16 v[192:195], v[48:51], v[176:179], v[192:195]\n" \
  "v_mul_f32 v217, s47, v217\n" \
  "v_mfma_f32_16x16x32_bf16 v[196:199], v[80:83], v[176:179], v[196:199]\n" \
  "v_mul_f32 v218, s47, v218\n" \
  "v_mfma_f32_16x16x32_bf16 v[200:203], v[112:115], v[176:179], v[200:203]\n" \
  "v_mul_f32 v219, s47, v219\n" \
  "v_mfma_f32_16x16x32_bf16 v[204:207], v[144:147], v[176:179], v[204:207]\n" \
  "ds_read_b128 v[176:179], v232 offset:256\n" \
  "v_mul_f32 v220, s47, v220\n" \
  "v_mfma_f32_16x16x32_bf16 v[192:195], v[52:55], v[180:183], v[192:195]\n" \
  "v_mul_f32 v221, s47, v221\n" \
  "v_mfma_f32_16x16x32_bf16 v[196:199], v[84:87], v[180:183], v[196:199]\n" \
  "v_mul_f32 v222, s47, v222\n" \
  "v_mfma_f32_16x16x32_bf16 v[200:203], v[116:119], v[180:183], v[200:203]\n" \
  "v_mul_f32 v223, s47, v223\n" \
  "v_mfma_f32_16x16x32_bf16 v[204:207], v[148:151], v[180:183], v[204:207]\n" \
  "ds_read_b128 v[180:183], v233 offset:256\n" \
  "s_waitcnt lgkmcnt(6)\n" \
  "v_cvt_pk_bf16_f32 v228, v216, v217\n" \
  "v_mfma_f32_16x16x32_bf16 v[192:195], v[56:59], v[184:187], v[192:195]\n" \
  "v_cvt_pk_bf16_f32 v229, v218, v219\n" \
  "v_add_u32 v232, s46, v232\n" \
  "v_mfma_f32_16x16x32_bf16 v[196:199], v[88:91], v[184:187], v[196:199]\n" \
  "v_cvt_pk_bf16_f32 v230, v220, v221\n" \
  "v_add_u32 v233, s46, v233\n" \
  "v_mfma_f32_16x16x32_bf16 v[200:203], v[120:123], v[184:187], v[200:203]\n" \
  "v_cvt_pk_bf16_f32 v231, v222, v223\n" \
  "v_mfma_f32_16x16x32_bf16 v[204:207], v[152:155], v[184:187], v[204:207]\n" \
  "ds_read_b128 v[184:187], v234 offset:256\n" \
  "buffer_store_dwordx4 v[228:231], v239, %[rw], s44 offen sc1\n" \
  "v_mfma_f32_16x16x32_bf16 v[208:211], v[60:63], v[188:191], v[192:195]\n" \
  "v_mov_b32 v238, %[st]\n" \
  "s_add_u32 s44, s44, 14336\n" \
  "v_mfma_f32_16x16x32_bf16 v[212:215], v[92:95], v[188:191], v[196:199]\n" \
  "v_add_u32 v239, s41, %[st]\n" \
  "s_sub_u32 s42, s42, 1\n" \
  "v_mfma_f32_16x16x32_bf16 v[216:219], v[124:127], v[188:191], v[200:203]\n" \
  "v_mfma_f32_16x16x32_bf16 v[220:223], v[156:159], v[188:191], v[204:207]\n" \
  "ds_read_b128 v[188:191], v235 offset:256\n" \
  "s_waitcnt lgkmcnt(6)\n" \
  "s_cbranch_scc0 52b\n" \
  "s_nop 7\n" \
  "v_mul_f32 v208, s47, v208\n" \
  "v_mul_f32 v209, s47, v209\n" \
  "v_mul_f32 v210, s47, v210\n" \
  "v_mul_f32 v211, s47, v211\n" \
  "v_mul_f32 v212, s47, v212\n" \
  "v_mul_f32 v213, s47, v213\n" \
  "v_mul_f32 v214, s47, v214\n" \
  "v_mul_f32 v215, s47, v215\n" \
  "v_cvt_pk_bf16_f32 v224, v208, v209\n" \
  "v_cvt_pk_bf16_f32 v225, v210, v211\n" \
  "v_cvt_pk_bf16_f32 v226, v212, v213\n" \
  "v_cvt_pk_bf16_f32 v227, v214, v215\n" \
  "buffer_store_dwordx4 v[224:227], v238, %[rw], s44 offen sc1\n" \
  "v_mul_f32 v216, s47, v216\n" \
  "v_mul_f32 v217, s47, v217\n" \
  "v_mul_f32 v218, s47, v218\n" \
  "v_mul_f32 v219, s47, v219\n" \
  "v_mul_f32 v220, s47, v220\n" \
  "v_mul_f32 v221, s47, v221\n" \
  "v_mul_f32 v222, s47, v222\n" \
  "v_mul_f32 v223, s47, v223\n" \
  "v_cvt_pk_bf16_f32 v228, v216, v217\n" \
  "v_cvt_pk_bf16_f32 v229, v218, v219\n" \
  "v_cvt_pk_bf16_f32 v230, v220, v221\n" \
  "v_cvt_pk_bf16_f32 v231, v222, v223\n" \
  "buffer_store_dwordx4 v[228:231], v239, %[rw], s44 offen sc1\n" \
  "s_branch 69f\n" \
  "60:\n" \
  "s_mov_b32 s46, 32\n" \
  "s_sub_u32 s44, %[fb], 229376\n" \
  "v_mov_b32 v232, %[tb]\n" \
  "ds_read_b64_tr_b16 v[160:161], v232 offset:0\n" \
  "ds_read_b64_tr_b16 v[162:163], v232 offset:1792\n" \
  "ds_read_b64_tr_b16 v[164:165], v232 offset:7168\n" \
  "ds_read_b64_tr_b16 v[166:167], v232 offset:8960\n" \
  "ds_read_b64_tr_b16 v[168:169], v232 offset:14336\n" \
  "ds_read_b64_tr_b16 v[170:171], v232 offset:16128\n" \
  "ds_read_b64_tr_b16 v[172:173], v232 offset:21504\n" \
  "ds_read_b64_tr_b16 v[174:175], v232 offset:23296\n" \
  "ds_read_b64_tr_b16 v[176:177], v232 offset:28672\n" \
  "ds_read_b64_tr_b16 v[178:179], v232 offset:30464\n" \
  "ds_read_b64_tr_b16 v[180:181], v232 offset:35840\n" \
  "ds_read_b64_tr_b16 v[182:183], v232 offset:37632\n" \
  "ds_read_b64_tr_b16 v[184:185], v232 offset:43008\n" \
  "ds_read_b64_tr_b16 v[186:187], v232 offset:44800\n" \
  "ds_read_b64_tr_b16 v[188:189], v232 offset:50176\n" \
  "ds_read_b64_tr_b16 v[190:191], v232 offset:51968\n" \
  "v_bfrev_b32 v238, 1\n" \
  "v_bfrev_b32 v239, 1\n" \
  "s_mov_b32 s42, 6\n" \
  "s_waitcnt lgkmcnt(12)\n" \
  "62:\n" \
  "v_mfma_f32_16x16x32_bf16 v[192:195], v[32:35], v[160:163], 0\n" \
  "v_add_u32 v232, s46, v232\n" \
  "v_mfma_f32_16x16x32_bf16 v[196:199], v[64:67], v[160:163], 0\n" \
  "v_mfma_f32_16x16x32_bf16 v[200:203], v[96:99], v[160:163], 0\n" \
  "v_mfma_f32_16x16x32_bf16 v[204:207], v[128:131], v[160:163], 0\n" \
  "ds_read_b64_tr_b16 v[160:161], v232 offset:0\n" \
  "ds_read_b64_tr_b16 v[162:163], v232 offset:1792\n" \
  "v_mfma_f32_16x16x32_bf16 v[192:195], v[36:39], v[164:167], v[192:195]\n" \
  "v_cvt_pk_bf16_f32 v224, v208, v209\n" \
  "v_mfma_f32_16x16x32_bf16 v[196:199], v[68:71], v[164:167], v[196:199]\n" \
  "v_cvt_pk_bf16_f32 v225, v210, v211\n" \
  "v_mfma_f32_16x16x32_bf16 v[200:203], v[100:103], v[164:167], v[200:203]\n" \
  "v_cvt_pk_bf16_f32 v226, v212, v213\n" \
  "v_mfma_f32_16x16x32_bf16 v[204:207], v[132:135], v[164:167], v[204:207]\n" \
  "ds_read_b64_tr_b16 v[164:165], v232 offset:7168\n" \
  "ds_read_b64_tr_b16 v[166:167], v232 offset:8960\n" \
  "s_waitcnt lgkmcnt(12)\n" \
  "v_cvt_pk_bf16_f32 v227, v214, v215\n" \
  "v_mfma_f32_16x16x32_bf16 v[192:195], v[40:43], v[168:171], v[192:195]\n" \
  "buffer_store_dwordx4 v[224:227], v238, %[rw], s44 offen sc1\n" \
  "v_mfma_f32_16x16x32_bf16 v[196:199], v[72:75], v[168:171], v[196:199]\n" \
  "v_mfma_f32_16x16x32_bf16 v[200:203], v[104:107], v[168:171], v[200:203]\n" \
  "v_mfma_f32_16x16x32_bf16 v[204:207], v[136:139], v[168:171], v[204:207]\n" \
  "ds_read_b64_tr_b16 v[168:169], v232 offset:14336\n" \
  "ds_read_b64_tr_b16 v[170:171], v232 offset:16128\n" \
  "v_mfma_f32_16x16x32_bf16 v[192:195], v[44:47], v[172:175], v[192:195]\n" \
  "v_mfma_f32_16x16x32_bf16 v[196:199], v[76:79], v[172:175], v[196:199]\n" \
  "v_mfma_f32_16x16x32_bf16 v[200:203], v[108:111], v[172:175], v[200:203]\n" \
  "v_mfma_f32_16x16x32_bf16 v[204:207], v[140:143], v[172:175], v[204:207]\n" \
  "ds_read_b64_tr_b16 v[172:173], v232 offset:21504\n" \
  "ds_read_b64_tr_b16 v[174:175], v232 offset:23296\n" \
  "s_waitcnt lgkmcnt(12)\n" \
  "v_mfma_f32_16x16x32_bf16 v[192:195], v[48:51], v[176:179], v[192:195]\n" \
  "v_cvt_pk_bf16_f32 v228, v216, v217\n" \
  "v_mfma_f32_16x16x32_bf16 v[196:199], v[80:83], v[176:179], v[196:199]\n" \
  "v_cvt_pk_bf16_f32 v229, v218, v219\n" \
  "v_mfma_f32_16x16x32_bf16 v[200:203], v[112:115], v[176:179], v[200:203]\n" \
  "v_cvt_pk_bf16_f32 v230, v220, v221\n" \
  "v_mfma_f32_16x16x32_bf16 v[204:207], v[144:147], v[176:179], v[204:207]\n" \
  "ds_read_b64_tr_b16 v[176:177], v232 offset:28672\n" \
  "ds_read_b64_tr_b16 v[178:179], v232 offset:30464\n" \
  "v_cvt_pk_bf16_f32 v231, v222, v223\n" \
  "v_mfma_f32_16x16x32_bf16 v[192:195], v[52:55], v[180:183], v[192:195]\n" \
  "buffer_store_dwordx4 v[228:231], v239, %[rw], s44 offen sc1\n" \
  "v_mfma_f32_16x16x32_bf16 v[196:199], v[84:87], v[180:183], v[196:199]\n" \
  "v_mfma_f32_16x16x32_bf16 v[200:203], v[116:119], v[180:183], v[200:203]\n" \
  "v_mfma_f32_16x16x32_bf16 v[204:207], v[148:151], v[180:183], v[204:207]\n" \
  "ds_read_b64_tr_b16 v[180:181], v232 offset:35840\n" \
  "ds_read_b64_tr_b16 v[182:183], v232 offset:37632\n" \
  "s_waitcnt lgkmcnt(12)\n" \
  "v_mfma_f32_16x16x32_bf16 v[192:195], v[56:59], v[184:187], v[192:195]\n" \
  "v_mfma_f32_16x16x32_bf16 v[196:199], v[88:91], v[184:187], v[196:199]\n" \
  "v_mfma_f32_16x16x32_bf16 v[200:203], v[120:123], v[184:187], v[200:203]\n" \
  "v_mfma_f32_16x16x32_bf16 v[204:207], v[152:155], v[184:187], v[204:207]\n" \
  "ds_read_b64_tr_b16 v[184:185], v232 offset:43008\n" \
  "ds_read_b64_tr_b16 v[186:187], v232 offset:44800\n" \
  "v_mfma_f32_16x16x32_bf16 v[208:211], v[60:63], v[188:191], v[192:195]\n" \
  "v_mov_b32 v238, %[st]\n" \
  "s_add_u32 s44, s44, 229376\n" \
  "v_mfma_f32_16x16x32_bf16 v[212:215], v[92:95], v[188:191], v[196:199]\n" \
  "v_add_u32 v239, s41, %[st]\n" \
  "s_sub_u32 s42, s42, 1\n" \
  "v_mfma_f32_16x16x32_bf16 v[216:219], v[124:127], v[188:191], v[200:203]\n" \
  "v_mfma_f32_16x16x32_bf16 v[220:223], v[156:159], v[188:191], v[204:207]\n" \
  "ds_read_b64_tr_b16 v[188:189], v232 offset:50176\n" \
  "ds_read_b64_tr_b16 v[190:191], v232 offset:51968\n" \
  "s_waitcnt lgkmcnt(12)\n" \
  "s_cbranch_scc0 62b\n" \
  "s_nop 7\n" \
  "v_cvt_pk_bf16_f32 v224, v208, v209\n" \
  "v_cvt_pk_bf16_f32 v225, v210, v211\n" \
  "v_cvt_pk_bf16_f32 v226, v212, v213\n" \
  "v_cvt_pk_bf16_f32 v227, v214, v215\n" \
  "buffer_store_dwordx4 v[224:227], v238, %[rw], s44 offen sc1\n" \
  "s_nop 1\n" \
  "v_cvt_pk_bf16_f32 v228, v216, v217\n" \
  "v_cvt_pk_bf16_f32 v229, v218, v219\n" \
  "v_cvt_pk_bf16_f32 v230, v220, v221\n" \
  "v_cvt_pk_bf16_f32 v231, v222, v223\n" \
  "buffer_store_dwordx4 v[228:231], v239, %[rw], s44 offen sc1\n" \
  "69:\n" \
  "s_waitcnt vmcnt(0) lgkmcnt(0)\n" \
  "s_barrier\n"
#define ASM_CLOBP "v32", "v33", "v34", "v35", "v36", "v37", "v38", "v39", "v40", "v41", "v42", "v43", "v44", "v45", "v46", "v47", "v48", "v49", "v50", "v51", "v52", "v53", "v54", "v55", "v56", "v57", "v58", "v59", "v60", "v61", "v62", "v63", "v64", "v65", "v66", "v67", "v68", "v69", "v70", "v71", "v72", "v73", "v74", "v75", "v76", "v77", "v78", "v79", "v80", "v81", "v82", "v83", "v84", "v85", "v86", "v87", "v88", "v89", "v90", "v91", "v92", "v93", "v94", "v95", "v96", "v97", "v98", "v99", "v100", "v101", "v102", "v103", "v104", "v105", "v106", "v107", "v108", "v109", "v110", "v111", "v112", "v113", "v114", "v115", "v116", "v117", "v118", "v119", "v120", "v121", "v122", "v123", "v124", "v125", "v126", "v127", "v128", "v129", "v130", "v131", "v132", "v133", "v134", "v135", "v136", "v137", "v138", "v139", "v140", "v141", "v142", "v143", "v144", "v145", "v146", "v147", "v148", "v149", "v150", "v151", "v152", "v153", "v154", "v155", "v156", "v157", "v158", "v159", "v160", "v161", "v162", "v163", "v164", "v165", "v166", "v167", "v168", "v169", "v170", "v171", "v172", "v173", "v174", "v175", "v176", "v177", "v178", "v179", "v180", "v181", "v182", "v183", "v184", "v185", "v186", "v187", "v188", "v189", "v190", "v191", "v192", "v193", "v194", "v195", "v196", "v197", "v198", "v199", "v200", "v201", "v202", "v203", "v204", "v205", "v206", "v207", "v208", "v209", "v210", "v211", "v212", "v213", "v214", "v215", "v216", "v217", "v218", "v219", "v220", "v221", "v222", "v223", "v224", "v225", "v226", "v227", "v228", "v229", "v230", "v231", "v232", "v233", "v234", "v235", "v236", "v237", "v238", "v239", "v240", "v241", "v242", "v243", "v244", "v245", "v246", "v247", "s40", "s41", "s42", "s43", "s44", "s45", "s46", "s47", "s48", "s49", "scc", "m0", "memory"

DEVI void produce(Ctx& c, const void* Wq, const void* Wk, const void* Wv, const void* Wo, int& role, int& a0, int& a1) {
  const int g = blockIdx.x, l = lane_id(), w = c.w;
  const int id = (g & 7) * 4 + (g >> 6), piece = (g >> 3) & 7;
  const int mat = id >> 4, h = id & 15, rh = piece >> 2, cq = piece & 3;
  const int n = l & 15, q = l >> 4, hi = l >> 5;
  const u32 WBYTES = 448u * 4096u * 2u;
  const rsrc_t rw = c.rw;
  const u32 LB = lds_addr(c.lds);
  const rsrc_t rb = mk(mat ? Wo : Wk, WBYTES), ra = mk(mat ? Wv : Wq, WBYTES);
  const u32 ab = (u32)(32 * (7 * rh + w)) * 8192u + (u32)h * 512u;
  const u32 oa = ab + hi * 8192 + (((l & 31) ^ hi) << 4);
  asm volatile(ASM_P1 :: [oa] "v"(oa), [ra] "s"(ra), [la] "s"(LB + 57344 + w * 32768), [wv] "s"(w) : ASM_CLOBP);
  u32 tp0 = RTP;
  if (mat) {
#pragma unroll
    for (int i = 0; i < 7; ++i) {
      const int t = i * 64 + l, r = t / 14, ch = t - r * 14, e = (r & 19) | (r & 4) << 1 | (r & 8) >> 1;
      const u32 ob = (u32)(h * 256 + w * 64 + e) * 896u + cq * 224 + ch * 16;
      dma16i(rb, LB + w * 14336, i * 1024, ob, 0);
      dma16i(rb, LB + w * 14336, i * 1024 + 7168, ob, 32 * 896);
    }
  } else {
#pragma unroll
    for (int i = 0; i < 14; ++i) {
      const int nn = 8 * (i & 1) + 2 * w + hi;
      dma16i(rb, LB + w * 1024, i * 4096, (u32)colmap(cq, i >> 1, nn) * 8192u + (u32)(h * 512) + (u32)(((l & 31) ^ nn) << 4), 0);
    }
  }
  const u2 tag = __builtin_amdgcn_raw_buffer_load_b64(rw, 0, OFF_PW, SC1);
  u32 tp1 = RTP;
  int bid = g;
  asm volatile("" : "+s"(bid));
  role = roleof(0, bid, a0, a1);
  u32 k[24];
#pragma unroll
  for (int i = 0; i < 24; ++i) k[i] = 0;
  if (role == 1) consts_S(c, a0, k);
  if (role == 0) consts_V(c, a0, k);
#pragma unroll
  for (int i = 0; i < 24; ++i) asm volatile("" : "+v"(k[i]));
  c.isS = role; c.isV = role; c.isO = role; c.isX = role;
  asm volatile("" : "+s"(role), "+s"(a0), "+s"(a1), "+s"(c.isS), "+s"(c.isV), "+s"(c.isO), "+s"(c.isX));
  u32 ro[4];
#pragma unroll
  for (int e = 0; e < 4; ++e) ro[e] = (u32)(n * 512 + (((4 * e + q) ^ n) << 4));
  const int np = 2 * (n >> 3) + ((n >> 2) & 1) + 4 * ((n >> 1) & 1) + 8 * (n & 1);
  u32 tp2 = RTP;
  asm volatile(ASM_P
               :: [oa] "v"(oa), [xp] "v"(l < 56 ? (u32)(a1 * 224 + w * 56 + l) * 128u : 0x80000000u), [rx] "s"(c.rx), [od] "v"(ab + (u32)(8 * (n >> 2) + (n & 3) + 4) * 8192u + q * 16),
                  [ro0] "v"(ro[0]), [ro1] "v"(ro[1]), [ro2] "v"(ro[2]), [ro3] "v"(ro[3]), [st] "v"((q * 16 + (mat ? np : n)) * 16),
                  [tb] "v"(LB + (16 * (q >> 1) + 4 * (q & 1) + (n >> 2)) * 224 + (n & 3) * 8),
                  [ra] "s"(ra), [rw] "s"(rw), [lb] "s"(LB), [la] "s"(LB + 57344 + w * 32768), [wv] "s"(w), [mat] "s"(mat),
                  [rtv] "s"(w == 0 ? blockIdx.x * 32u + OFF_DBG + 73728u : 0x80000000u),
                  [fb] "s"((mat ? OFF_WB + (u32)(7 * cq * 16 + h) * 14336u : OFF_WA + (u32)(h * 4 + cq) * 7u * 14336u) + (u32)(7 * rh + w) * 1024u)
               : ASM_CLOBP);
  c.e0 = uni(tag[0]) + 1; c.e1 = uni(tag[1]);
  if (w == 0) st8c(rw, l == 0 ? (u32)(id * 8 + piece) * 128u : 0x80000000u, OFF_F1, (u2){c.e0, c.e1});
  if ((u32)role < 2u) {
#pragma unroll
    for (int i = 0; i < 6; ++i) *(u4*)(c.lds + L_CST + (w * 6 + i) * 1024 + l * 16) = (u4){k[4 * i], k[4 * i + 1], k[4 * i + 2], k[4 * i + 3]};
  }
}
DEVI void done2(const Ctx& c, int u) {
  asm volatile("s_waitcnt vmcnt(0)\n\ts_barrier" ::: "memory");
  if (c.w == 0) st8c(c.rw, lane_id() == 0 ? (u32)(256 + u) * 128u : 0x80000000u, OFF_F1, (u2){c.e0, c.e1});
}
#define NI 47
DEVI void role_I(const Ctx& c, int u, const void* mem0) {
  const int l = lane_id(), w = c.w;
  const rsrc_t rm = mk(mem0, 7168u);
  const int mi = u * 64 + l, ll = (mi >> 2) & 63, rr = ll & 15;
  const u32 mo = (u < 28 && w == 0) ? (u32)mi * 4u : 0x80000000u;
  const u32 m0a = __builtin_amdgcn_raw_buffer_load_b32(rm, ((rr >> 1) * 224 + 32 * (mi >> 8) + 8 * (ll >> 4) + 2 * (mi & 3) + (rr & 1)) * 4 | (mo & 0x80000000u), 0, 0);
  const u4 ones = {~0u, ~0u, ~0u, ~0u};
  constexpr int N0 = 1024, N1 = N0 + 64 * 24, N2 = N1 + 120, N3 = N2 + (OFF_INIT_END - OFF_KVF) / 1024;
  for (int k = u * 4 + w; k < N3; k += 4 * NI) {
    const int p = k - N0;
    const u32 so = k < N0 ? OFF_MEMA + k * 1024 : (k < N1 ? OFF_P + (u32)(p / 6 * 8 + p % 6) * 1024u : (k < N2 ? OFF_PX - N1 * 1024 : OFF_KVF - N2 * 1024) + (u32)k * 1024u);
    if (k >= N0 || (k >= 16 && (k & 7) != 7)) st16c(c.rw, l * 16, so, ones);
  }
  st4c(c.rw, mo, OFF_MEMA, nosent(m0a));
  st4c(c.rw, mo, OFF_MEMA + 8192, nosent(m0a));
  done2(c, u);
}
DEVI void wait_pieces(const Ctx& c, u32 o, bool on) {
  for (int sp = 0; sp < SPIN_MAX; ++sp) {
    cbar();
    const u2 f = __builtin_amdgcn_raw_buffer_load_b64(c.rw, on ? o : 0x80000000u, OFF_F1, SC1);
    if (__builtin_amdgcn_ballot_w64(on && (f[0] != c.e0 || f[1] != c.e1)) == 0) break;
    __builtin_amdgcn_s_sleep(FPOLL);
  }
  cbar();
}
DEVI u32 wait_go(const Ctx& c) {
  u2 f;
  for (int sp = 0; sp < SPIN_MAX; ++sp) {
    cbar();
    f = __builtin_amdgcn_raw_buffer_load_b64(c.rw, 0, OFF_PW, SC1);
    if (uni(f[0]) == c.e0) break;
    __builtin_amdgcn_s_sleep(4);
  }
  cbar();
  return uni(f[1]);
}

#define XTS 0
#define OTS 0
#define KUP 0
#define KDN 0
#define KOPS(m) [kup] "n"(KUP), [kdn] "n"(-m * KDN)
#define YBASE (OFF_Y + (u32)j * 16384u)
#define RTV_S RTV_V
#define RTV_V (w == 0 ? blockIdx.x * 32u + OFF_DBG + 73728u : 0x80000000u)
#define XFLAG (u32)(8192 + (h >> 3) * 2048 + (h & 7) * 128)
#define FTH_S 1000
#define FTH_V 200
#define NKS 4
#define NKV 8
#define DBGH 5
#define DBGJ 9
#define ASM_S \
  "v_mbcnt_lo_u32_b32 v116, -1, 0\n" \
  "v_mbcnt_hi_u32_b32 v116, -1, v116\n" \
  "v_lshlrev_b32 v116, 4, v116\n" \
  "v_add_u32 v117, %[cst], v116\n" \
  "ds_read_b128 v[0:3], v117 offset:0\n" \
  "ds_read_b128 v[4:7], v117 offset:1024\n" \
  "ds_read_b128 v[8:11], v117 offset:2048\n" \
  "ds_read_b128 v[12:15], v117 offset:3072\n" \
  "ds_read_b128 v[16:19], v117 offset:4096\n" \
  "ds_read_b128 v[20:23], v117 offset:5120\n" \
  "s_bfe_u32 s48, %[kbs], 0x40000\n" \
  "s_lshl_b32 s48, s48, 11\n" \
  "s_add_u32 s48, s48, %[wb]\n" \
  "s_bfe_u32 s49, %[kbs], 0x40004\n" \
  "s_lshl_b32 s49, s49, 11\n" \
  "s_add_u32 s49, s49, %[wb]\n" \
  "s_bfe_u32 s50, %[kbs], 0x40008\n" \
  "s_lshl_b32 s50, s50, 11\n" \
  "s_add_u32 s50, s50, %[wb]\n" \
  "s_bfe_u32 s51, %[kbs], 0x4000c\n" \
  "s_lshl_b32 s51, s51, 11\n" \
  "s_add_u32 s51, s51, %[wb]\n" \
  "s_bfe_u32 s52, %[kbs], 0x40010\n" \
  "s_lshl_b32 s52, s52, 11\n" \
  "s_add_u32 s52, s52, %[wb]\n" \
  "s_bfe_u32 s53, %[kbs], 0x40014\n" \
  "s_lshl_b32 s53, s53, 11\n" \
  "s_add_u32 s53, s53, %[wb]\n" \
  "s_bfe_u32 s54, %[kbs], 0x40018\n" \
  "s_lshl_b32 s54, s54, 11\n" \
  "s_add_u32 s54, s54, %[wb]\n" \
  "s_add_u32 s40, s48, 0\n" \
  "buffer_load_dwordx4 a[0:3], v116, %[rw], s40 offen sc1\n" \
  "buffer_load_dwordx4 a[4:7], v116, %[rw], s40 offen offset:1024 sc1\n" \
  "s_add_u32 s40, s48, 14336\n" \
  "buffer_load_dwordx4 a[56:59], v116, %[rw], s40 offen sc1\n" \
  "buffer_load_dwordx4 a[60:63], v116, %[rw], s40 offen offset:1024 sc1\n" \
  "s_add_u32 s40, s48, 28672\n" \
  "buffer_load_dwordx4 a[112:115], v116, %[rw], s40 offen sc1\n" \
  "buffer_load_dwordx4 a[116:119], v116, %[rw], s40 offen offset:1024 sc1\n" \
  "s_add_u32 s40, s48, 43008\n" \
  "buffer_load_dwordx4 a[168:171], v116, %[rw], s40 offen sc1\n" \
  "buffer_load_dwordx4 a[172:175], v116, %[rw], s40 offen offset:1024 sc1\n" \
  "s_add_u32 s40, s48, 57344\n" \
  "buffer_load_dwordx4 a[224:227], v116, %[rw], s40 offen sc1\n" \
  "buffer_load_dwordx4 a[228:231], v116, %[rw], s40 offen offset:1024 sc1\n" \
  "s_add_u32 s40, s48, 71680\n" \
  "buffer_load_dwordx4 v[144:147], v116, %[rw], s40 offen sc1\n" \
  "buffer_load_dwordx4 v[148:151], v116, %[rw], s40 offen offset:1024 sc1\n" \
  "s_add_u32 s40, s48, 86016\n" \
  "buffer_load_dwordx4 v[200:203], v116, %[rw], s40 offen sc1\n" \
  "buffer_load_dwordx4 v[204:207], v116, %[rw], s40 offen offset:1024 sc1\n" \
  "s_add_u32 s40, s49, 0\n" \
  "buffer_load_dwordx4 a[8:11], v116, %[rw], s40 offen sc1\n" \
  "buffer_load_dwordx4 a[12:15], v116, %[rw], s40 offen offset:1024 sc1\n" \
  "s_add_u32 s40, s49, 14336\n" \
  "buffer_load_dwordx4 a[64:67], v116, %[rw], s40 offen sc1\n" \
  "buffer_load_dwordx4 a[68:71], v116, %[rw], s40 offen offset:1024 sc1\n" \
  "s_add_u32 s40, s49, 28672\n" \
  "buffer_load_dwordx4 a[120:123], v116, %[rw], s40 offen sc1\n" \
  "buffer_load_dwordx4 a[124:127], v116, %[rw], s40 offen offset:1024 sc1\n" \
  "s_add_u32 s40, s49, 43008\n" \
  "buffer_load_dwordx4 a[176:179], v116, %[rw], s40 offen sc1\n" \
  "buffer_load_dwordx4 a[180:183], v116, %[rw], s40 offen offset:1024 sc1\n" \
  "s_add_u32 s40, s49, 57344\n" \
  "buffer_load_dwordx4 a[232:235], v116, %[rw], s40 offen sc1\n" \
  "buffer_load_dwordx4 a[236:239], v116, %[rw], s40 offen offset:1024 sc1\n" \
  "s_add_u32 s40, s49, 71680\n" \
  "buffer_load_dwordx4 v[152:155], v116, %[rw], s40 offen sc1\n" \
  "buffer_load_dwordx4 v[156:159], v116, %[rw], s40 offen offset:1024 sc1\n" \
  "s_add_u32 s40, s49, 86016\n" \
  "buffer_load_dwordx4 v[208:211], v116, %[rw], s40 offen sc1\n" \
  "buffer_load_dwordx4 v[212:215], v116, %[rw], s40 offen offset:1024 sc1\n" \
  "s_add_u32 s40, s50, 0\n" \
  "buffer_load_dwordx4 a[16:19], v116, %[rw], s40 offen sc1\n" \
  "buffer_load_dwordx4 a[20:23], v116, %[rw], s40 offen offset:1024 sc1\n" \
  "s_add_u32 s40, s50, 14336\n" \
  "buffer_load_dwordx4 a[72:75], v116, %[rw], s40 offen sc1\n" \
  "buffer_load_dwordx4 a[76:79], v116, %[rw], s40 offen offset:1024 sc1\n" \
  "s_add_u32 s40, s50, 28672\n" \
  "buffer_load_dwordx4 a[128:131], v116, %[rw], s40 offen sc1\n" \
  "buffer_load_dwordx4 a[132:135], v116, %[rw], s40 offen offset:1024 sc1\n" \
  "s_add_u32 s40, s50, 43008\n" \
  "buffer_load_dwordx4 a[184:187], v116, %[rw], s40 offen sc1\n" \
  "buffer_load_dwordx4 a[188:191], v116, %[rw], s40 offen offset:1024 sc1\n" \
  "s_add_u32 s40, s50, 57344\n" \
  "buffer_load_dwordx4 a[240:243], v116, %[rw], s40 offen sc1\n" \
  "buffer_load_dwordx4 a[244:247], v116, %[rw], s40 offen offset:1024 sc1\n" \
  "s_add_u32 s40, s50, 71680\n" \
  "buffer_load_dwordx4 v[160:163], v116, %[rw], s40 offen sc1\n" \
  "buffer_load_dwordx4 v[164:167], v116, %[rw], s40 offen offset:1024 sc1\n" \
  "s_add_u32 s40, s50, 86016\n" \
  "buffer_load_dwordx4 v[216:219], v116, %[rw], s40 offen sc1\n" \
  "buffer_load_dwordx4 v[220:223], v116, %[rw], s40 offen offset:1024 sc1\n" \
  "s_add_u32 s40, s51, 0\n" \
  "buffer_load_dwordx4 a[24:27], v116, %[rw], s40 offen sc1\n" \
  "buffer_load_dwordx4 a[28:31], v116, %[rw], s40 offen offset:1024 sc1\n" \
  "s_add_u32 s40, s51, 14336\n" \
  "buffer_load_dwordx4 a[80:83], v116, %[rw], s40 offen sc1\n" \
  "buffer_load_dwordx4 a[84:87], v116, %[rw], s40 offen offset:1024 sc1\n" \
  "s_add_u32 s40, s51, 28672\n" \
  "buffer_load_dwordx4 a[136:139], v116, %[rw], s40 offen sc1\n" \
  "buffer_load_dwordx4 a[140:143], v116, %[rw], s40 offen offset:1024 sc1\n" \
  "s_add_u32 s40, s51, 43008\n" \
  "buffer_load_dwordx4 a[192:195], v116, %[rw], s40 offen sc1\n" \
  "buffer_load_dwordx4 a[196:199], v116, %[rw], s40 offen offset:1024 sc1\n" \
  "s_add_u32 s40, s51, 57344\n" \
  "buffer_load_dwordx4 a[248:251], v116, %[rw], s40 offen sc1\n" \
  "buffer_load_dwordx4 a[252:255], v116, %[rw], s40 offen offset:1024 sc1\n" \
  "s_add_u32 s40, s51, 71680\n" \
  "buffer_load_dwordx4 v[168:171], v116, %[rw], s40 offen sc1\n" \
  "buffer_load_dwordx4 v[172:175], v116, %[rw], s40 offen offset:1024 sc1\n" \
  "s_add_u32 s40, s51, 86016\n" \
  "buffer_load_dwordx4 v[224:227], v116, %[rw], s40 offen sc1\n" \
  "buffer_load_dwordx4 v[228:231], v116, %[rw], s40 offen offset:1024 sc1\n" \
  "s_add_u32 s40, s52, 0\n" \
  "buffer_load_dwordx4 a[32:35], v116, %[rw], s40 offen sc1\n" \
  "buffer_load_dwordx4 a[36:39], v116, %[rw], s40 offen offset:1024 sc1\n" \
  "s_add_u32 s40, s52, 14336\n" \
  "buffer_load_dwordx4 a[88:91], v116, %[rw], s40 offen sc1\n" \
  "buffer_load_dwordx4 a[92:95], v116, %[rw], s40 offen offset:1024 sc1\n" \
  "s_add_u32 s40, s52, 28672\n" \
  "buffer_load_dwordx4 a[144:147], v116, %[rw], s40 offen sc1\n" \
  "buffer_load_dwordx4 a[148:151], v116, %[rw], s40 offen offset:1024 sc1\n" \
  "s_add_u32 s40, s52, 43008\n" \
  "buffer_load_dwordx4 a[200:203], v116, %[rw], s40 offen sc1\n" \
  "buffer_load_dwordx4 a[204:207], v116, %[rw], s40 offen offset:1024 sc1\n" \
  "s_add_u32 s40, s52, 57344\n" \
  "buffer_load_dwordx4 v[120:123], v116, %[rw], s40 offen sc1\n" \
  "buffer_load_dwordx4 v[124:127], v116, %[rw], s40 offen offset:1024 sc1\n" \
  "s_add_u32 s40, s52, 71680\n" \
  "buffer_load_dwordx4 v[176:179], v116, %[rw], s40 offen sc1\n" \
  "buffer_load_dwordx4 v[180:183], v116, %[rw], s40 offen offset:1024 sc1\n" \
  "s_add_u32 s40, s52, 86016\n" \
  "buffer_load_dwordx4 v[232:235], v116, %[rw], s40 offen sc1\n" \
  "buffer_load_dwordx4 v[236:239], v116, %[rw], s40 offen offset:1024 sc1\n" \
  "s_add_u32 s40, s53, 0\n" \
  "buffer_load_dwordx4 a[40:43], v116, %[rw], s40 offen sc1\n" \
  "buffer_load_dwordx4 a[44:47], v116, %[rw], s40 offen offset:1024 sc1\n" \
  "s_add_u32 s40, s53, 14336\n" \
  "buffer_load_dwordx4 a[96:99], v116, %[rw], s40 offen sc1\n" \
  "buffer_load_dwordx4 a[100:103], v116, %[rw], s40 offen offset:1024 sc1\n" \
  "s_add_u32 s40, s53, 28672\n" \
  "buffer_load_dwordx4 a[152:155], v116, %[rw], s40 offen sc1\n" \
  "buffer_load_dwordx4 a[156:159], v116, %[rw], s40 offen offset:1024 sc1\n" \
  "s_add_u32 s40, s53, 43008\n" \
  "buffer_load_dwordx4 a[208:211], v116, %[rw], s40 offen sc1\n" \
  "buffer_load_dwordx4 a[212:215], v116, %[rw], s40 offen offset:1024 sc1\n" \
  "s_add_u32 s40, s53, 57344\n" \
  "buffer_load_dwordx4 v[128:131], v116, %[rw], s40 offen sc1\n" \
  "buffer_load_dwordx4 v[132:135], v116, %[rw], s40 offen offset:1024 sc1\n" \
  "s_add_u32 s40, s53, 71680\n" \
  "buffer_load_dwordx4 v[184:187], v116, %[rw], s40 offen sc1\n" \
  "buffer_load_dwordx4 v[188:191], v116, %[rw], s40 offen offset:1024 sc1\n" \
  "s_add_u32 s40, s53, 86016\n" \
  "buffer_load_dwordx4 v[240:243], v116, %[rw], s40 offen sc1\n" \
  "buffer_load_dwordx4 v[244:247], v116, %[rw], s40 offen offset:1024 sc1\n" \
  "s_add_u32 s40, s54, 0\n" \
  "buffer_load_dwordx4 a[48:51], v116, %[rw], s40 offen sc1\n" \
  "buffer_load_dwordx4 a[52:55], v116, %[rw], s40 offen offset:1024 sc1\n" \
  "s_add_u32 s40, s54, 14336\n" \
  "buffer_load_dwordx4 a[104:107], v116, %[rw], s40 offen sc1\n" \
  "buffer_load_dwordx4 a[108:111], v116, %[rw], s40 offen offset:1024 sc1\n" \
  "s_add_u32 s40, s54, 28672\n" \
  "buffer_load_dwordx4 a[160:163], v116, %[rw], s40 offen sc1\n" \
  "buffer_load_dwordx4 a[164:167], v116, %[rw], s40 offen offset:1024 sc1\n" \
  "s_add_u32 s40, s54, 43008\n" \
  "buffer_load_dwordx4 a[216:219], v116, %[rw], s40 offen sc1\n" \
  "buffer_load_dwordx4 a[220:223], v116, %[rw], s40 offen offset:1024 sc1\n" \
  "s_add_u32 s40, s54, 57344\n" \
  "buffer_load_dwordx4 v[136:139], v116, %[rw], s40 offen sc1\n" \
  "buffer_load_dwordx4 v[140:143], v116, %[rw], s40 offen offset:1024 sc1\n" \
  "s_add_u32 s40, s54, 71680\n" \
  "buffer_load_dwordx4 v[192:195], v116, %[rw], s40 offen sc1\n" \
  "buffer_load_dwordx4 v[196:199], v116, %[rw], s40 offen offset:1024 sc1\n" \
  "s_add_u32 s40, s54, 86016\n" \
  "buffer_load_dwordx4 v[248:251], v116, %[rw], s40 offen sc1\n" \
  "buffer_load_dwordx4 v[252:255], v116, %[rw], s40 offen offset:1024 sc1\n" \
  "s_waitcnt lgkmcnt(0)\n" \
  "v_mov_b32 v118, 0\n" \
  "v_mov_b32 v119, 0\n" \
  "s_mov_b32 s41, %[sm]\n" \
  "s_mov_b32 s42, %[sp]\n" \
  "s_mov_b32 s43, 0\n" \
  "s_mov_b32 s44, 63\n" \
  "s_mov_b32 s46, %[sm]\n" \
  "s_mov_b32 s45, %[spin]\n" \
  "v_mov_b32 v94, 0\n" \
  "s_mov_b32 s40, %[pw]\n" \
  "20:\n" \
  "buffer_load_dwordx2 v[92:93], v94, %[rw], s40 offen sc1\n" \
  "s_waitcnt vmcnt(0)\n" \
  "v_readfirstlane_b32 s49, v92\n" \
  "v_readfirstlane_b32 s50, v93\n" \
  "s_sub_u32 s45, s45, 1\n" \
  "s_cbranch_scc1 21f\n" \
  "s_cmp_lg_u32 s49, %[e0]\n" \
  "s_cbranch_scc1 20b\n" \
  "21:\n" \
  "s_and_b32 s49, s50, 8192\n" \
  "s_add_u32 s41, s41, s49\n" \
  "s_lshr_b32 s50, s50, 1\n" \
  "s_and_b32 s50, s50, 8192\n" \
  "s_add_u32 s42, s42, s50\n" \
  "s_mov_b32 s49, %[spin]\n" \
  "s_mov_b32 s40, 110\n" \
  "s_mov_b32 s47, 0\n" \
  "s_mov_b32 s86, 0\n" \
  "s_mov_b32 s48, %[slp]\n" \
  ".p2align 6\n" \
  "1:\n" \
  "v_mbcnt_lo_u32_b32 v92, -1, 0\n" \
  "v_mbcnt_hi_u32_b32 v92, -1, v92\n" \
  "v_cmp_gt_u32 vcc, 3, v92\n" \
  "v_lshl_add_u32 v93, v92, 2, %[dbo]\n" \
  "v_bfrev_b32 v117, 1\n" \
  "v_cndmask_b32 v117, v117, v93, vcc\n" \
  "s_bitcmp0_b32 %[dbo], 10\n" \
  "s_cbranch_scc1 35f\n" \
  "s_mov_b32 s50, s80\n" \
  "s_mov_b32 s87, s55\n" \
  "s_sub_u32 s83, s82, s55\n" \
  "s_cmp_gt_i32 s83, %[fth]\n" \
  "s_cselect_b32 s82, 160, 0\n" \
  "s_memtime s[52:53]\n" \
  "s_nop 0\n" \
  "s_memrealtime s[54:55]\n" \
  "s_nop 0\n" \
  "s_memrealtime s[56:57]\n" \
  "s_nop 0\n" \
  "s_memrealtime s[58:59]\n" \
  "s_nop 0\n" \
  "s_memrealtime s[60:61]\n" \
  "s_nop 0\n" \
  "s_memrealtime s[62:63]\n" \
  "s_nop 0\n" \
  "s_memrealtime s[64:65]\n" \
  "s_nop 0\n" \
  "s_memrealtime s[66:67]\n" \
  "s_nop 0\n" \
  "s_memrealtime s[68:69]\n" \
  "s_nop 0\n" \
  "s_memrealtime s[70:71]\n" \
  "s_nop 0\n" \
  "s_memrealtime s[72:73]\n" \
  "s_nop 0\n" \
  "s_memrealtime s[74:75]\n" \
  "s_nop 0\n" \
  "s_memrealtime s[76:77]\n" \
  "s_waitcnt lgkmcnt(0)\n" \
  "s_add_u32 s54, s54, s56\n" \
  "s_add_u32 s54, s54, s58\n" \
  "s_add_u32 s54, s54, s60\n" \
  "s_add_u32 s54, s54, s62\n" \
  "s_add_u32 s54, s54, s64\n" \
  "s_add_u32 s54, s54, s66\n" \
  "s_add_u32 s54, s54, s68\n" \
  "s_add_u32 s54, s54, s70\n" \
  "s_add_u32 s54, s54, s72\n" \
  "s_add_u32 s54, s54, s74\n" \
  "s_add_u32 s54, s54, s76\n" \
  "s_lshl_b32 s81, s54, 1\n" \
  "s_sub_u32 s80, s52, s81\n" \
  "s_add_u32 s55, s52, 4096\n" \
  "s_cmp_eq_u32 s44, 63\n" \
  "s_cbranch_scc1 35f\n" \
  "s_sub_u32 s57, s50, s80\n" \
  "s_sub_i32 s57, s57, s40\n" \
  "s_add_i32 s57, s57, 2\n" \
  "s_ashr_i32 s57, s57, 2\n" \
  "s_max_i32 s57, s57, -16\n" \
  "s_min_i32 s57, s57, 16\n" \
  "s_add_i32 s40, s40, s57\n" \
  "s_mov_b32 s45, %[spin]-1\n" \
  "s_waitcnt vmcnt(1)\n" \
  "s_branch 34f\n" \
  "30:\n" \
  "buffer_load_dword v100, v3, %[rw], s46 offen sc1\n" \
  "s_sub_u32 s45, s45, 1\n" \
  "s_waitcnt vmcnt(0)\n" \
  "s_cbranch_scc1 37f\n" \
  "34:\n" \
  "v_cmp_eq_u32 vcc, -1, v100\n" \
  "s_cmp_lg_u32 vcc_lo, 0\n" \
  "s_cbranch_scc1 30b\n" \
  "37:\n" \
  "v_readlane_b32 s47, v100, 48\n" \
  "v_subrev_u32 v100, s81, v100\n" \
  "s_nop 1\n" \
  "v_max_i32_dpp v100, v100, v100 row_ror:8 row_mask:0xf bank_mask:0xf\n" \
  "s_nop 1\n" \
  "v_max_i32_dpp v100, v100, v100 row_ror:4 row_mask:0xf bank_mask:0xf\n" \
  "s_nop 1\n" \
  "v_max_i32_dpp v100, v100, v100 row_ror:2 row_mask:0xf bank_mask:0xf\n" \
  "s_nop 1\n" \
  "v_max_i32_dpp v100, v100, v100 row_ror:1 row_mask:0xf bank_mask:0xf\n" \
  "s_add_u32 s56, s50, s81\n" \
  "s_nop 0\n" \
  "v_readfirstlane_b32 s55, v100\n" \
  "v_readlane_b32 s57, v100, 16\n" \
  "s_not_b32 s47, s47\n" \
  "s_and_b32 s48, s47, 0xff\n" \
  "s_sub_u32 s48, %[slp], s48\n" \
  "s_bfe_u32 s47, s47, 0x80008\n" \
  "s_add_u32 s53, s55, s56\n" \
  "s_sub_i32 s53, s53, s87\n" \
  "s_cmp_gt_i32 s53, %[fth]\n" \
  "s_cselect_b32 s53, 0, 0\n" \
  "s_cmp_lt_u32 s44, 59\n" \
  "s_cselect_b32 s53, s53, 0\n" \
  "s_add_u32 s86, s86, s53\n" \
  "s_min_u32 s86, s86, 60\n" \
  "s_add_u32 s48, s48, s86\n" \
  "s_lshr_b32 s53, s86, 1\n" \
  "s_sub_i32 s47, s47, s53\n" \
  "s_add_i32 s55, s57, s82\n" \
  "s_min_i32 s55, s55, 8000\n" \
  "s_add_u32 s55, s55, s56\n" \
  "s_add_u32 m0, %[xm0], 0\n" \
  "s_nop 0\n" \
  "buffer_load_dwordx4 v7, %[rx], s43 offen offset:0 lds\n" \
  "s_add_u32 m0, %[xm0], 896\n" \
  "s_nop 0\n" \
  "buffer_load_dwordx4 v7, %[rx], s43 offen offset:128 lds\n" \
  "s_add_u32 m0, %[xm0], 1792\n" \
  "s_nop 0\n" \
  "buffer_load_dwordx4 v7, %[rx], s43 offen offset:256 lds\n" \
  "s_add_u32 m0, %[xm0], 2688\n" \
  "s_nop 0\n" \
  "buffer_load_dwordx4 v7, %[rx], s43 offen offset:384 lds\n" \
  "s_add_u32 m0, %[xm0], 3584\n" \
  "s_nop 0\n" \
  "buffer_load_dwordx4 v7, %[rx], s43 offen offset:512 lds\n" \
  "s_add_u32 m0, %[xm0], 4480\n" \
  "s_nop 0\n" \
  "buffer_load_dwordx4 v7, %[rx], s43 offen offset:640 lds\n" \
  "s_add_u32 m0, %[xm0], 5376\n" \
  "s_nop 0\n" \
  "buffer_load_dwordx4 v7, %[rx], s43 offen offset:768 lds\n" \
  "s_sub_u32 s57, 0, s80\n" \
  "v_mov_b32 v116, s57\n" \
  "s_sub_u32 s57, s48, s80\n" \
  "v_writelane_b32 v116, s57, 1\n" \
  "s_sub_u32 s57, s40, s80\n" \
  "s_sub_u32 s57, s57, s47\n" \
  "v_writelane_b32 v116, s57, 0\n" \
  "31:\n" \
  "s_memtime s[52:53]\n" \
  "s_waitcnt lgkmcnt(0)\n" \
  "s_sub_i32 s56, s55, s52\n" \
  "s_sub_u32 s53, s56, 125\n" \
  "s_cmp_lt_u32 s53, 16384\n" \
  "s_cbranch_scc1 31b\n" \
  "s_max_i32 s56, s56, 0\n" \
  "s_and_b32 s56, s56, 0x7c\n" \
  "s_getpc_b64 s[58:59]\n" \
  "32:\n" \
  "s_sub_u32 s56, 33f-32b, s56\n" \
  "s_add_u32 s58, s58, s56\n" \
  "s_addc_u32 s59, s59, 0\n" \
  "s_setpc_b64 s[58:59]\n" \
  "s_nop 0\n" \
  "s_nop 0\n" \
  "s_nop 0\n" \
  "s_nop 0\n" \
  "s_nop 0\n" \
  "s_nop 0\n" \
  "s_nop 0\n" \
  "s_nop 0\n" \
  "s_nop 0\n" \
  "s_nop 0\n" \
  "s_nop 0\n" \
  "s_nop 0\n" \
  "s_nop 0\n" \
  "s_nop 0\n" \
  "s_nop 0\n" \
  "s_nop 0\n" \
  "s_nop 0\n" \
  "s_nop 0\n" \
  "s_nop 0\n" \
  "s_nop 0\n" \
  "s_nop 0\n" \
  "s_nop 0\n" \
  "s_nop 0\n" \
  "s_nop 0\n" \
  "s_nop 0\n" \
  "s_nop 0\n" \
  "s_nop 0\n" \
  "s_nop 0\n" \
  "s_nop 0\n" \
  "s_nop 0\n" \
  "s_nop 0\n" \
  "33:\n" \
  "s_branch 36f\n" \
  "35:\n" \
  "s_add_u32 m0, %[xm0], 0\n" \
  "s_nop 0\n" \
  "buffer_load_dwordx4 v7, %[rx], s43 offen offset:0 lds\n" \
  "s_add_u32 m0, %[xm0], 896\n" \
  "s_nop 0\n" \
  "buffer_load_dwordx4 v7, %[rx], s43 offen offset:128 lds\n" \
  "s_add_u32 m0, %[xm0], 1792\n" \
  "s_nop 0\n" \
  "buffer_load_dwordx4 v7, %[rx], s43 offen offset:256 lds\n" \
  "s_add_u32 m0, %[xm0], 2688\n" \
  "s_nop 0\n" \
  "buffer_load_dwordx4 v7, %[rx], s43 offen offset:384 lds\n" \
  "s_add_u32 m0, %[xm0], 3584\n" \
  "s_nop 0\n" \
  "buffer_load_dwordx4 v7, %[rx], s43 offen offset:512 lds\n" \
  "s_add_u32 m0, %[xm0], 4480\n" \
  "s_nop 0\n" \
  "buffer_load_dwordx4 v7, %[rx], s43 offen offset:640 lds\n" \
  "s_add_u32 m0, %[xm0], 5376\n" \
  "s_nop 0\n" \
  "buffer_load_dwordx4 v7, %[rx], s43 offen offset:768 lds\n" \
  "s_bitcmp1_b32 %[dbo], 13\n" \
  "s_cbranch_scc0 37f\n" \
  "s_cmp_gt_u32 s44, 5\n" \
  "s_cbranch_scc1 37f\n" \
  "s_memtime s[52:53]\n" \
  "s_memrealtime s[56:57]\n" \
  "s_waitcnt lgkmcnt(0)\n" \
  "s_mul_i32 s57, s56, 24\n" \
  "s_sub_u32 s80, s52, s57\n" \
  "37:\n" \
  "s_sub_u32 s57, 0, s80\n" \
  "v_mov_b32 v116, s57\n" \
  "s_sub_u32 s57, s48, s80\n" \
  "v_writelane_b32 v116, s57, 1\n" \
  "s_sub_u32 s57, s40, s80\n" \
  "s_sub_u32 s57, s57, s47\n" \
  "v_writelane_b32 v116, s57, 0\n" \
  ".p2align 6\n" \
  "36:\n" \
  "s_barrier\n" \
  "2:\n" \
  "buffer_load_dwordx4 v[24:27], v1, %[rw], s41 offen sc1\n" \
  "s_nop 10\n" \
  "buffer_load_dwordx4 v[28:31], v2, %[rw], s41 offen sc1\n" \
  "s_nop 7\n" \
  "v_mov_b32 v52, 0\n" \
  "v_mov_b32 v53, 0\n" \
  "v_mov_b32 v54, 0\n" \
  "v_mov_b32 v55, 0\n" \
  "v_mov_b32 v56, 0\n" \
  "v_mov_b32 v57, 0\n" \
  "v_mov_b32 v58, 0\n" \
  "v_mov_b32 v59, 0\n" \
  "v_mov_b32 v60, 0\n" \
  "v_mov_b32 v61, 0\n" \
  "v_mov_b32 v62, 0\n" \
  "v_mov_b32 v63, 0\n" \
  "v_mov_b32 v64, 0\n" \
  "v_mov_b32 v65, 0\n" \
  "v_mov_b32 v66, 0\n" \
  "v_mov_b32 v67, 0\n" \
  "v_mov_b32 v68, 0\n" \
  "v_mov_b32 v69, 0\n" \
  "v_mov_b32 v70, 0\n" \
  "v_mov_b32 v71, 0\n" \
  "v_mov_b32 v72, 0\n" \
  "v_mov_b32 v73, 0\n" \
  "v_mov_b32 v74, 0\n" \
  "v_mov_b32 v75, 0\n" \
  "v_mov_b32 v76, 0\n" \
  "v_mov_b32 v77, 0\n" \
  "v_mov_b32 v78, 0\n" \
  "v_mov_b32 v79, 0\n" \
  "v_mov_b32 v80, 0\n" \
  "v_mov_b32 v81, 0\n" \
  "v_mov_b32 v82, 0\n" \
  "v_mov_b32 v83, 0\n" \
  "v_mov_b32 v84, 0\n" \
  "v_mov_b32 v85, 0\n" \
  "v_mov_b32 v86, 0\n" \
  "v_mov_b32 v87, 0\n" \
  "v_mov_b32 v88, 0\n" \
  "v_mov_b32 v89, 0\n" \
  "v_mov_b32 v90, 0\n" \
  "v_mov_b32 v91, 0\n" \
  "s_waitcnt vmcnt(1)\n" \
  "v_smfmac_f32_16x16x64_bf16 v[52:55], v[24:27], a[0:7], v0\n" \
  "ds_write_b64 v1, v[24:25] offset:28672\n" \
  "v_max3_u32 v92, v24, v25, v26\n" \
  "v_smfmac_f32_16x16x64_bf16 v[56:59], v[24:27], a[56:63], v0\n" \
  "ds_write_b64 v1, v[26:27] offset:28680\n" \
  "v_smfmac_f32_16x16x64_bf16 v[60:63], v[24:27], a[112:119], v0\n" \
  "v_smfmac_f32_16x16x64_bf16 v[64:67], v[24:27], a[168:175], v0\n" \
  "s_waitcnt vmcnt(0)\n" \
  "ds_write_b64 v1, v[28:29] offset:29696\n" \
  "v_max3_u32 v92, v92, v27, v28\n" \
  "v_smfmac_f32_16x16x64_bf16 v[68:71], v[24:27], a[224:231], v0\n" \
  "ds_write_b64 v1, v[30:31] offset:29704\n" \
  "v_max3_u32 v92, v92, v29, v30\n" \
  "v_smfmac_f32_16x16x64_bf16 v[72:75], v[24:27], v[144:151], v0\n" \
  "v_max_u32 v92, v92, v31\n" \
  "v_smfmac_f32_16x16x64_bf16 v[76:79], v[24:27], v[200:207], v0\n" \
  "v_cmp_eq_u32 vcc, -1, v92\n" \
  "v_smfmac_f32_16x16x64_bf16 v[52:55], v[28:31], a[8:15], v0\n" \
  "s_cbranch_vccnz 4f\n" \
  "3:\n" \
  "v_smfmac_f32_16x16x64_bf16 v[56:59], v[28:31], a[64:71], v0\n" \
  "s_waitcnt lgkmcnt(0)\n" \
  "s_barrier\n" \
  "v_smfmac_f32_16x16x64_bf16 v[60:63], v[28:31], a[120:127], v0\n" \
  "ds_read_b128 v[32:35], v4\n" \
  "v_smfmac_f32_16x16x64_bf16 v[64:67], v[28:31], a[176:183], v0\n" \
  "s_memtime s[82:83]\n" \
  "ds_read_b128 v[36:39], v4 offset:1024\n" \
  "v_smfmac_f32_16x16x64_bf16 v[68:71], v[28:31], a[232:239], v0\n" \
  "ds_read_b128 v[40:43], v5\n" \
  "v_smfmac_f32_16x16x64_bf16 v[72:75], v[28:31], v[152:159], v0\n" \
  "ds_read_b128 v[44:47], v5 offset:1024\n" \
  "v_smfmac_f32_16x16x64_bf16 v[76:79], v[28:31], v[208:215], v0\n" \
  "ds_read_b128 v[48:51], v6\n" \
  "s_waitcnt lgkmcnt(4)\n" \
  "v_smfmac_f32_16x16x64_bf16 v[52:55], v[32:35], a[16:23], v0\n" \
  "s_waitcnt lgkmcnt(3)\n" \
  "v_add_u32 v116, s82, v116\n" \
  "v_smfmac_f32_16x16x64_bf16 v[52:55], v[36:39], a[24:31], v0\n" \
  "buffer_store_dword v116, v117, %[rw], s42 offen sc1\n" \
  "s_waitcnt lgkmcnt(2)\n" \
  "v_smfmac_f32_16x16x64_bf16 v[52:55], v[40:43], a[32:39], v0\n" \
  "s_waitcnt lgkmcnt(1)\n" \
  "v_smfmac_f32_16x16x64_bf16 v[52:55], v[44:47], a[40:47], v0\n" \
  "s_waitcnt lgkmcnt(0)\n" \
  "v_smfmac_f32_16x16x64_bf16 v[52:55], v[48:51], a[48:55], v0\n" \
  "ds_read_b128 v[92:95], v8 offset:0\n" \
  "v_smfmac_f32_16x16x64_bf16 v[56:59], v[32:35], a[72:79], v0\n" \
  "ds_read_b128 v[96:99], v9 offset:0\n" \
  "v_smfmac_f32_16x16x64_bf16 v[56:59], v[36:39], a[80:87], v0\n" \
  "ds_read_b128 v[100:103], v8 offset:14336\n" \
  "v_cvt_pk_bf16_f32 v52, v52, v53\n" \
  "v_smfmac_f32_16x16x64_bf16 v[56:59], v[40:43], a[88:95], v0\n" \
  "ds_read_b128 v[104:107], v9 offset:14336\n" \
  "v_cvt_pk_bf16_f32 v53, v54, v55\n" \
  "v_smfmac_f32_16x16x64_bf16 v[56:59], v[44:47], a[96:103], v0\n" \
  "ds_read2_b32 v[108:109], v12 offset0:0 offset1:4\n" \
  "v_smfmac_f32_16x16x64_bf16 v[56:59], v[48:51], a[104:111], v0\n" \
  "v_smfmac_f32_16x16x64_bf16 v[60:63], v[32:35], a[128:135], v0\n" \
  "ds_read2_b32 v[110:111], v12 offset0:1 offset1:5\n" \
  "v_smfmac_f32_16x16x64_bf16 v[60:63], v[36:39], a[136:143], v0\n" \
  "v_cvt_pk_bf16_f32 v54, v56, v57\n" \
  "v_smfmac_f32_16x16x64_bf16 v[60:63], v[40:43], a[144:151], v0\n" \
  "ds_read2_b32 v[112:113], v13 offset0:0 offset1:4\n" \
  "v_cvt_pk_bf16_f32 v55, v58, v59\n" \
  "v_smfmac_f32_16x16x64_bf16 v[60:63], v[44:47], a[152:159], v0\n" \
  "v_mfma_f32_16x16x32_bf16 v[56:59], v[52:55], v[20:23], 0\n" \
  "ds_read2_b32 v[114:115], v13 offset0:1 offset1:5\n" \
  "v_smfmac_f32_16x16x64_bf16 v[60:63], v[48:51], a[160:167], v0\n" \
  "v_smfmac_f32_16x16x64_bf16 v[64:67], v[32:35], a[184:191], v0\n" \
  "ds_read2_b32 v[24:25], v14 offset0:0 offset1:4\n" \
  "v_cvt_pk_bf16_f32 v52, v56, v57\n" \
  "v_smfmac_f32_16x16x64_bf16 v[64:67], v[36:39], a[192:199], v0\n" \
  "v_cvt_pk_bf16_f32 v53, v58, v59\n" \
  "v_smfmac_f32_16x16x64_bf16 v[64:67], v[40:43], a[200:207], v0\n" \
  "ds_read2_b32 v[26:27], v14 offset0:1 offset1:5\n" \
  "v_cvt_pk_bf16_f32 v60, v60, v61\n" \
  "v_smfmac_f32_16x16x64_bf16 v[64:67], v[44:47], a[208:215], v0\n" \
  "v_cvt_pk_bf16_f32 v61, v62, v63\n" \
  "v_smfmac_f32_16x16x64_bf16 v[64:67], v[48:51], a[216:223], v0\n" \
  "ds_read2_b32 v[28:29], v15 offset0:0 offset1:4\n" \
  "v_smfmac_f32_16x16x64_bf16 v[68:71], v[32:35], a[240:247], v0\n" \
  "v_smfmac_f32_16x16x64_bf16 v[68:71], v[36:39], a[248:255], v0\n" \
  "ds_read2_b32 v[30:31], v15 offset0:1 offset1:5\n" \
  "v_cvt_pk_bf16_f32 v62, v64, v65\n" \
  "v_smfmac_f32_16x16x64_bf16 v[68:71], v[40:43], v[120:127], v0\n" \
  "v_cvt_pk_bf16_f32 v63, v66, v67\n" \
  "v_smfmac_f32_16x16x64_bf16 v[68:71], v[44:47], v[128:135], v0\n" \
  "v_mfma_f32_16x16x32_bf16 v[64:67], v[60:63], v[20:23], 0\n" \
  "v_smfmac_f32_16x16x64_bf16 v[68:71], v[48:51], v[136:143], v0\n" \
  "v_smfmac_f32_16x16x64_bf16 v[72:75], v[32:35], v[160:167], v0\n" \
  "v_cvt_pk_bf16_f32 v54, v64, v65\n" \
  "v_smfmac_f32_16x16x64_bf16 v[72:75], v[36:39], v[168:175], v0\n" \
  "s_waitcnt lgkmcnt(0)\n" \
  "v_cvt_pk_bf16_f32 v55, v66, v67\n" \
  "v_smfmac_f32_16x16x64_bf16 v[72:75], v[40:43], v[176:183], v0\n" \
  "v_cvt_pk_bf16_f32 v68, v68, v69\n" \
  "v_smfmac_f32_16x16x64_bf16 v[72:75], v[44:47], v[184:191], v0\n" \
  "ds_read_b128 v[60:63], v10 offset:0\n" \
  "v_cvt_pk_bf16_f32 v69, v70, v71\n" \
  "v_smfmac_f32_16x16x64_bf16 v[80:83], v[52:55], v[92:99], v0\n" \
  "ds_read_b128 v[64:67], v11 offset:0\n" \
  "v_smfmac_f32_16x16x64_bf16 v[72:75], v[48:51], v[192:199], v0\n" \
  "ds_read_b128 v[92:95], v10 offset:14336\n" \
  "v_smfmac_f32_16x16x64_bf16 v[76:79], v[32:35], v[216:223], v0\n" \
  "ds_read_b128 v[96:99], v11 offset:14336\n" \
  "v_smfmac_f32_16x16x64_bf16 v[76:79], v[36:39], v[224:231], v0\n" \
  "v_cvt_pk_bf16_f32 v70, v72, v73\n" \
  "v_smfmac_f32_16x16x64_bf16 v[76:79], v[40:43], v[232:239], v0\n" \
  "v_cvt_pk_bf16_f32 v71, v74, v75\n" \
  "v_smfmac_f32_16x16x64_bf16 v[76:79], v[44:47], v[240:247], v0\n" \
  "v_smfmac_f32_16x16x64_bf16 v[76:79], v[48:51], v[248:255], v0\n" \
  "v_smfmac_f32_16x16x64_bf16 v[84:87], v[52:55], v[100:107], v0\n" \
  "buffer_load_dword v100, v3, %[rw], s42 offen sc1\n" \
  "v_mfma_f32_16x16x32_bf16 v[72:75], v[68:71], v[20:23], 0\n" \
  "v_cvt_pk_bf16_f32 v116, v76, v77\n" \
  "v_cvt_pk_bf16_f32 v117, v78, v79\n" \
  "s_nop 1\n" \
  "v_mfma_f32_16x16x32_bf16 v[76:79], v[116:119], v[20:23], 0\n" \
  "v_smfmac_f32_16x16x64_bf16 v[88:91], v[52:55], v[108:115], v0\n" \
  "s_waitcnt lgkmcnt(0)\n" \
  "s_nop 0\n" \
  "v_cvt_pk_bf16_f32 v68, v72, v73\n" \
  "v_cvt_pk_bf16_f32 v69, v74, v75\n" \
  "v_cvt_pk_bf16_f32 v70, v76, v77\n" \
  "v_cvt_pk_bf16_f32 v71, v78, v79\n" \
  "s_nop 1\n" \
  "v_smfmac_f32_16x16x64_bf16 v[80:83], v[68:71], v[60:67], v0 abid:1\n" \
  "v_smfmac_f32_16x16x64_bf16 v[84:87], v[68:71], v[92:99], v0 abid:1\n" \
  "v_smfmac_f32_16x16x64_bf16 v[88:91], v[68:71], v[24:31], v0 abid:1\n" \
  "s_nop 1\n" \
  "v_add_f32 v80, v80, v81\n" \
  "v_add_f32 v82, v82, v83\n" \
  "v_add_f32 v84, v84, v85\n" \
  "v_add_f32 v86, v86, v87\n" \
  "ds_write2_b32 v16, v80, v82 offset0:8 offset1:48\n" \
  "ds_write2_b32 v16, v84, v86 offset0:24 offset1:64\n" \
  "v_add_f32 v88, v88, v89\n" \
  "v_add_f32 v90, v90, v91\n" \
  "ds_write2_b32 v17, v88, v90 offset0:0 offset1:40\n" \
  "s_waitcnt lgkmcnt(0)\n" \
  "s_barrier\n" \
  "ds_read_b64 v[32:33], v18 offset:0\n" \
  "ds_read_b64 v[34:35], v18 offset:1312\n" \
  "ds_read_b64 v[36:37], v18 offset:2624\n" \
  "ds_read_b64 v[38:39], v18 offset:3936\n" \
  "s_waitcnt lgkmcnt(2)\n" \
  "v_pk_add_f32 v[32:33], v[32:33], v[34:35]\n" \
  "s_waitcnt lgkmcnt(1)\n" \
  "v_pk_add_f32 v[32:33], v[32:33], v[36:37]\n" \
  "s_waitcnt lgkmcnt(0)\n" \
  "v_pk_add_f32 v[32:33], v[32:33], v[38:39]\n" \
  "v_max_f32 v48, v32, v33\n" \
  "s_nop 1\n" \
  "v_max_f32_dpp v50, v48, v48 row_ror:4 row_mask:0xf bank_mask:0xf\n" \
  "v_max_f32_dpp v50, v48, v50 row_ror:8 row_mask:0xf bank_mask:0xf\n" \
  "v_max_f32_dpp v50, v48, v50 row_ror:12 row_mask:0xf bank_mask:0xf\n" \
  "s_nop 1\n" \
  "v_max_f32_dpp v48, v50, v50 row_ror:1 row_mask:0xf bank_mask:0xf\n" \
  "v_max_f32_dpp v48, v50, v48 row_ror:2 row_mask:0xf bank_mask:0xf\n" \
  "v_max_f32_dpp v51, v50, v48 row_ror:3 row_mask:0xf bank_mask:0xf\n" \
  "v_max_f32_dpp v48, v50, v48 row_ror:3 row_mask:0xf bank_mask:0xf\n" \
  "s_nop 1\n" \
  "v_permlane16_swap_b32 v48, v51\n" \
  "v_max_f32 v48, v48, v51\n" \
  "v_pk_add_f32 v[32:33], v[32:33], v[48:49] op_sel_hi:[1,0] neg_lo:[0,1] neg_hi:[0,1]\n" \
  "v_exp_f32 v32, v32\n" \
  "v_exp_f32 v33, v33\n" \
  "s_nop 0\n" \
  "v_add_f32 v48, v32, v33\n" \
  "s_nop 1\n" \
  "v_add_f32_dpp v50, v48, v48 row_ror:4 row_mask:0xf bank_mask:0xf\n" \
  "v_add_f32_dpp v50, v48, v50 row_ror:8 row_mask:0xf bank_mask:0xf\n" \
  "v_add_f32_dpp v50, v48, v50 row_ror:12 row_mask:0xf bank_mask:0xf\n" \
  "s_nop 1\n" \
  "v_add_f32_dpp v48, v50, v50 row_ror:1 row_mask:0xf bank_mask:0xf\n" \
  "v_add_f32_dpp v48, v50, v48 row_ror:2 row_mask:0xf bank_mask:0xf\n" \
  "v_add_f32_dpp v51, v50, v48 row_ror:3 row_mask:0xf bank_mask:0xf\n" \
  "v_add_f32_dpp v48, v50, v48 row_ror:3 row_mask:0xf bank_mask:0xf\n" \
  "s_nop 1\n" \
  "v_permlane16_swap_b32 v48, v51\n" \
  "v_add_f32 v48, v48, v51\n" \
  "v_rcp_f32 v48, v48\n" \
  "s_nop 0\n" \
  "v_pk_mul_f32 v[32:33], v[32:33], v[48:49] op_sel_hi:[1,0]\n" \
  "v_cvt_pk_bf16_f32 v32, v32, v33\n" \
  "v_min_u32 v32, -2, v32\n" \
  "buffer_store_dword v32, v19, %[rw], s42 offen sc1\n" \
  "s_mov_b32 s49, %[spin]\n" \
  "s_mov_b32 s46, s42\n" \
  "s_add_u32 s41, s41, 16384\n" \
  "s_add_u32 s42, s42, 32768\n" \
  "s_add_u32 s43, s43, 28672\n" \
  "s_sub_u32 s44, s44, 1\n" \
  "s_cmp_lg_u32 s44, 0\n" \
  "s_cbranch_scc1 1b\n" \
  "s_waitcnt vmcnt(0)\n" \
  "s_endpgm\n" \
  "4:\n" \
  "s_sub_u32 s49, s49, 1\n" \
  "s_cbranch_scc0 2b\n" \
  "s_branch 3b\n"
#define ASM_V \
  "v_mbcnt_lo_u32_b32 v116, -1, 0\n" \
  "v_mbcnt_hi_u32_b32 v116, -1, v116\n" \
  "v_lshlrev_b32 v116, 4, v116\n" \
  "v_add_u32 v117, %[cst], v116\n" \
  "ds_read_b128 v[0:3], v117 offset:0\n" \
  "ds_read_b128 v[4:7], v117 offset:1024\n" \
  "ds_read_b128 v[8:11], v117 offset:2048\n" \
  "ds_read_b128 v[12:15], v117 offset:3072\n" \
  "s_max_u32 s49, %[wv], 1\n" \
  "s_sub_u32 s49, s49, 1\n" \
  "s_lshl_b32 s49, s49, 12\n" \
  "s_add_u32 s50, %[wb], s49\n" \
  "s_add_u32 s40, s50, 0\n" \
  "buffer_load_dwordx4 a[0:3], v116, %[rw], s40 offen sc1\n" \
  "buffer_load_dwordx4 a[4:7], v116, %[rw], s40 offen offset:1024 sc1\n" \
  "s_add_u32 s40, s50, 14336\n" \
  "buffer_load_dwordx4 a[56:59], v116, %[rw], s40 offen sc1\n" \
  "buffer_load_dwordx4 a[60:63], v116, %[rw], s40 offen offset:1024 sc1\n" \
  "s_add_u32 s40, s50, 28672\n" \
  "buffer_load_dwordx4 a[112:115], v116, %[rw], s40 offen sc1\n" \
  "buffer_load_dwordx4 a[116:119], v116, %[rw], s40 offen offset:1024 sc1\n" \
  "s_add_u32 s40, s50, 43008\n" \
  "buffer_load_dwordx4 a[168:171], v116, %[rw], s40 offen sc1\n" \
  "buffer_load_dwordx4 a[172:175], v116, %[rw], s40 offen offset:1024 sc1\n" \
  "s_add_u32 s49, s49, 2048\n" \
  "s_cmp_eq_u32 s49, 14336\n" \
  "s_cselect_b32 s49, 0, s49\n" \
  "s_add_u32 s50, %[wb], s49\n" \
  "s_add_u32 s40, s50, 0\n" \
  "buffer_load_dwordx4 a[8:11], v116, %[rw], s40 offen sc1\n" \
  "buffer_load_dwordx4 a[12:15], v116, %[rw], s40 offen offset:1024 sc1\n" \
  "s_add_u32 s40, s50, 14336\n" \
  "buffer_load_dwordx4 a[64:67], v116, %[rw], s40 offen sc1\n" \
  "buffer_load_dwordx4 a[68:71], v116, %[rw], s40 offen offset:1024 sc1\n" \
  "s_add_u32 s40, s50, 28672\n" \
  "buffer_load_dwordx4 a[120:123], v116, %[rw], s40 offen sc1\n" \
  "buffer_load_dwordx4 a[124:127], v116, %[rw], s40 offen offset:1024 sc1\n" \
  "s_add_u32 s40, s50, 43008\n" \
  "buffer_load_dwordx4 a[176:179], v116, %[rw], s40 offen sc1\n" \
  "buffer_load_dwordx4 a[180:183], v116, %[rw], s40 offen offset:1024 sc1\n" \
  "s_add_u32 s49, s49, 2048\n" \
  "s_cmp_eq_u32 s49, 14336\n" \
  "s_cselect_b32 s49, 0, s49\n" \
  "s_add_u32 s50, %[wb], s49\n" \
  "s_add_u32 s40, s50, 0\n" \
  "buffer_load_dwordx4 a[16:19], v116, %[rw], s40 offen sc1\n" \
  "buffer_load_dwordx4 a[20:23], v116, %[rw], s40 offen offset:1024 sc1\n" \
  "s_add_u32 s40, s50, 14336\n" \
  "buffer_load_dwordx4 a[72:75], v116, %[rw], s40 offen sc1\n" \
  "buffer_load_dwordx4 a[76:79], v116, %[rw], s40 offen offset:1024 sc1\n" \
  "s_add_u32 s40, s50, 28672\n" \
  "buffer_load_dwordx4 a[128:131], v116, %[rw], s40 offen sc1\n" \
  "buffer_load_dwordx4 a[132:135], v116, %[rw], s40 offen offset:1024 sc1\n" \
  "s_add_u32 s40, s50, 43008\n" \
  "buffer_load_dwordx4 a[184:187], v116, %[rw], s40 offen sc1\n" \
  "buffer_load_dwordx4 a[188:191], v116, %[rw], s40 offen offset:1024 sc1\n" \
  "s_add_u32 s49, s49, 2048\n" \
  "s_cmp_eq_u32 s49, 14336\n" \
  "s_cselect_b32 s49, 0, s49\n" \
  "s_add_u32 s50, %[wb], s49\n" \
  "s_add_u32 s40, s50, 0\n" \
  "buffer_load_dwordx4 a[24:27], v116, %[rw], s40 offen sc1\n" \
  "buffer_load_dwordx4 a[28:31], v116, %[rw], s40 offen offset:1024 sc1\n" \
  "s_add_u32 s40, s50, 14336\n" \
  "buffer_load_dwordx4 a[80:83], v116, %[rw], s40 offen sc1\n" \
  "buffer_load_dwordx4 a[84:87], v116, %[rw], s40 offen offset:1024 sc1\n" \
  "s_add_u32 s40, s50, 28672\n" \
  "buffer_load_dwordx4 a[136:139], v116, %[rw], s40 offen sc1\n" \
  "buffer_load_dwordx4 a[140:143], v116, %[rw], s40 offen offset:1024 sc1\n" \
  "s_add_u32 s40, s50, 43008\n" \
  "buffer_load_dwordx4 a[192:195], v116, %[rw], s40 offen sc1\n" \
  "buffer_load_dwordx4 a[196:199], v116, %[rw], s40 offen offset:1024 sc1\n" \
  "s_add_u32 s49, s49, 2048\n" \
  "s_cmp_eq_u32 s49, 14336\n" \
  "s_cselect_b32 s49, 0, s49\n" \
  "s_add_u32 s50, %[wb], s49\n" \
  "s_add_u32 s40, s50, 0\n" \
  "buffer_load_dwordx4 a[32:35], v116, %[rw], s40 offen sc1\n" \
  "buffer_load_dwordx4 a[36:39], v116, %[rw], s40 offen offset:1024 sc1\n" \
  "s_add_u32 s40, s50, 14336\n" \
  "buffer_load_dwordx4 a[88:91], v116, %[rw], s40 offen sc1\n" \
  "buffer_load_dwordx4 a[92:95], v116, %[rw], s40 offen offset:1024 sc1\n" \
  "s_add_u32 s40, s50, 28672\n" \
  "buffer_load_dwordx4 a[144:147], v116, %[rw], s40 offen sc1\n" \
  "buffer_load_dwordx4 a[148:151], v116, %[rw], s40 offen offset:1024 sc1\n" \
  "s_add_u32 s40, s50, 43008\n" \
  "buffer_load_dwordx4 a[200:203], v116, %[rw], s40 offen sc1\n" \
  "buffer_load_dwordx4 a[204:207], v116, %[rw], s40 offen offset:1024 sc1\n" \
  "s_add_u32 s49, s49, 2048\n" \
  "s_cmp_eq_u32 s49, 14336\n" \
  "s_cselect_b32 s49, 0, s49\n" \
  "s_add_u32 s50, %[wb], s49\n" \
  "s_add_u32 s40, s50, 0\n" \
  "buffer_load_dwordx4 a[40:43], v116, %[rw], s40 offen sc1\n" \
  "buffer_load_dwordx4 a[44:47], v116, %[rw], s40 offen offset:1024 sc1\n" \
  "s_add_u32 s40, s50, 14336\n" \
  "buffer_load_dwordx4 a[96:99], v116, %[rw], s40 offen sc1\n" \
  "buffer_load_dwordx4 a[100:103], v116, %[rw], s40 offen offset:1024 sc1\n" \
  "s_add_u32 s40, s50, 28672\n" \
  "buffer_load_dwordx4 a[152:155], v116, %[rw], s40 offen sc1\n" \
  "buffer_load_dwordx4 a[156:159], v116, %[rw], s40 offen offset:1024 sc1\n" \
  "s_add_u32 s40, s50, 43008\n" \
  "buffer_load_dwordx4 a[208:211], v116, %[rw], s40 offen sc1\n" \
  "buffer_load_dwordx4 a[212:215], v116, %[rw], s40 offen offset:1024 sc1\n" \
  "s_add_u32 s49, s49, 2048\n" \
  "s_cmp_eq_u32 s49, 14336\n" \
  "s_cselect_b32 s49, 0, s49\n" \
  "s_add_u32 s50, %[wb], s49\n" \
  "s_add_u32 s40, s50, 0\n" \
  "buffer_load_dwordx4 a[48:51], v116, %[rw], s40 offen sc1\n" \
  "buffer_load_dwordx4 a[52:55], v116, %[rw], s40 offen offset:1024 sc1\n" \
  "s_add_u32 s40, s50, 14336\n" \
  "buffer_load_dwordx4 a[104:107], v116, %[rw], s40 offen sc1\n" \
  "buffer_load_dwordx4 a[108:111], v116, %[rw], s40 offen offset:1024 sc1\n" \
  "s_add_u32 s40, s50, 28672\n" \
  "buffer_load_dwordx4 a[160:163], v116, %[rw], s40 offen sc1\n" \
  "buffer_load_dwordx4 a[164:167], v116, %[rw], s40 offen offset:1024 sc1\n" \
  "s_add_u32 s40, s50, 43008\n" \
  "buffer_load_dwordx4 a[216:219], v116, %[rw], s40 offen sc1\n" \
  "buffer_load_dwordx4 a[220:223], v116, %[rw], s40 offen offset:1024 sc1\n" \
  "s_add_u32 s49, s49, 2048\n" \
  "s_cmp_eq_u32 s49, 14336\n" \
  "s_cselect_b32 s49, 0, s49\n" \
  "v_mov_b32 v24, 0\n" \
  "v_mov_b32 v25, 0\n" \
  "v_mov_b32 v26, 0\n" \
  "v_mov_b32 v27, 0\n" \
  "s_waitcnt lgkmcnt(0)\n" \
  "s_mov_b32 s41, %[sm]\n" \
  "s_mov_b32 s42, %[sp]\n" \
  "s_mov_b32 s43, %[sy]\n" \
  "s_mov_b32 s44, 63\n" \
  "s_mov_b32 s48, %[sf]\n" \
  "v_mov_b32 v188, -1\n" \
  "s_mov_b32 s45, %[spin]\n" \
  "v_mov_b32 v94, 0\n" \
  "s_mov_b32 s40, %[pw]\n" \
  "20:\n" \
  "buffer_load_dwordx2 v[92:93], v94, %[rw], s40 offen sc1\n" \
  "s_waitcnt vmcnt(0)\n" \
  "v_readfirstlane_b32 s49, v92\n" \
  "v_readfirstlane_b32 s50, v93\n" \
  "s_sub_u32 s45, s45, 1\n" \
  "s_cbranch_scc1 21f\n" \
  "s_cmp_lg_u32 s49, %[e0]\n" \
  "s_cbranch_scc1 20b\n" \
  "21:\n" \
  "s_and_b32 s49, s50, 8192\n" \
  "s_add_u32 s41, s41, s49\n" \
  "s_lshr_b32 s50, s50, 1\n" \
  "s_and_b32 s50, s50, 8192\n" \
  "s_add_u32 s42, s42, s50\n" \
  "s_cmp_lg_u32 %[wv], 0\n" \
  "s_cbranch_scc1 19f\n" \
  "buffer_load_dword v62, v7, %[rw], s41 offen sc1\n" \
  "s_waitcnt vmcnt(0)\n" \
  "ds_write_b32 v3, v62\n" \
  "19:\n" \
  "s_cmp_lg_u32 %[wv], 1\n" \
  "s_cbranch_scc1 18f\n" \
  "s_memtime s[52:53]\n" \
  "s_nop 0\n" \
  "s_memrealtime s[54:55]\n" \
  "s_nop 0\n" \
  "s_memrealtime s[56:57]\n" \
  "s_nop 0\n" \
  "s_memrealtime s[58:59]\n" \
  "s_nop 0\n" \
  "s_memrealtime s[60:61]\n" \
  "s_nop 0\n" \
  "s_memrealtime s[62:63]\n" \
  "s_nop 0\n" \
  "s_memrealtime s[64:65]\n" \
  "s_nop 0\n" \
  "s_memrealtime s[66:67]\n" \
  "s_nop 0\n" \
  "s_memrealtime s[68:69]\n" \
  "s_nop 0\n" \
  "s_memrealtime s[70:71]\n" \
  "s_nop 0\n" \
  "s_memrealtime s[72:73]\n" \
  "s_nop 0\n" \
  "s_memrealtime s[74:75]\n" \
  "s_nop 0\n" \
  "s_memrealtime s[76:77]\n" \
  "s_waitcnt lgkmcnt(0)\n" \
  "s_add_u32 s54, s54, s56\n" \
  "s_add_u32 s54, s54, s58\n" \
  "s_add_u32 s54, s54, s60\n" \
  "s_add_u32 s54, s54, s62\n" \
  "s_add_u32 s54, s54, s64\n" \
  "s_add_u32 s54, s54, s66\n" \
  "s_add_u32 s54, s54, s68\n" \
  "s_add_u32 s54, s54, s70\n" \
  "s_add_u32 s54, s54, s72\n" \
  "s_add_u32 s54, s54, s74\n" \
  "s_add_u32 s54, s54, s76\n" \
  "s_lshl_b32 s81, s54, 1\n" \
  "s_sub_u32 s80, s52, s81\n" \
  "18:\n" \
  "s_memtime s[78:79]\n" \
  "s_waitcnt lgkmcnt(0)\n" \
  "s_mov_b32 s50, s78\n" \
  "s_mov_b32 s51, %[slp2]\n" \
  "s_mov_b32 s46, 110\n" \
  ".p2align 6\n" \
  "1:\n" \
  "s_cmp_lg_u32 %[wv], 0\n" \
  "s_cbranch_scc1 11f\n" \
  "v_mov_b32 v44, 0\n" \
  "v_mov_b32 v45, 0\n" \
  "v_mov_b32 v46, 0\n" \
  "v_mov_b32 v47, 0\n" \
  "v_mov_b32 v48, 0\n" \
  "v_mov_b32 v49, 0\n" \
  "v_mov_b32 v50, 0\n" \
  "v_mov_b32 v51, 0\n" \
  "v_mov_b32 v52, 0\n" \
  "v_mov_b32 v53, 0\n" \
  "v_mov_b32 v54, 0\n" \
  "v_mov_b32 v55, 0\n" \
  "v_mov_b32 v56, 0\n" \
  "v_mov_b32 v57, 0\n" \
  "v_mov_b32 v58, 0\n" \
  "v_mov_b32 v59, 0\n" \
  "s_mov_b32 s45, %[spin]\n" \
  "v_cmp_eq_u32 vcc, -1, v188\n" \
  "s_cbranch_vccz 5f\n" \
  "4:\n" \
  "buffer_load_dword v188, v10, %[rw], s48 offen sc1\n" \
  "s_sub_u32 s45, s45, 1\n" \
  "s_cbranch_scc1 5f\n" \
  "s_waitcnt vmcnt(0)\n" \
  "v_cmp_eq_u32 vcc, -1, v188\n" \
  "s_cbranch_vccnz 4b\n" \
  "5:\n" \
  "s_add_u32 s49, s43, 4096\n" \
  "s_add_u32 s50, s43, 8192\n" \
  "s_add_u32 s51, s43, 12288\n" \
  "s_waitcnt lgkmcnt(0)\n" \
  "s_barrier\n" \
  "s_nop 15\n" \
  "s_nop 15\n" \
  "s_nop 3\n" \
  "buffer_load_dwordx4 v[64:67], v1, %[rw], s43 offen offset:0 sc1\n" \
  "s_nop 3\n" \
  "buffer_load_dwordx4 v[68:71], v1, %[rw], s43 offen offset:1024 sc1\n" \
  "s_nop 3\n" \
  "buffer_load_dwordx4 v[72:75], v1, %[rw], s43 offen offset:2048 sc1\n" \
  "s_nop 3\n" \
  "buffer_load_dwordx4 v[76:79], v1, %[rw], s43 offen offset:3072 sc1\n" \
  "s_nop 3\n" \
  "buffer_load_dwordx4 v[80:83], v1, %[rw], s49 offen offset:0 sc1\n" \
  "s_nop 3\n" \
  "buffer_load_dwordx4 v[84:87], v1, %[rw], s49 offen offset:1024 sc1\n" \
  "s_nop 3\n" \
  "buffer_load_dwordx4 v[88:91], v1, %[rw], s49 offen offset:2048 sc1\n" \
  "s_nop 3\n" \
  "buffer_load_dwordx4 v[92:95], v1, %[rw], s49 offen offset:3072 sc1\n" \
  "s_barrier\n" \
  "ds_read_b128 v[16:19], v5 offset:0\n" \
  "ds_read_b128 v[20:23], v5 offset:1024\n" \
  "ds_read_b128 v[24:27], v5 offset:2048\n" \
  "ds_read_b128 v[28:31], v5 offset:3072\n" \
  "ds_read_b128 v[32:35], v5 offset:4096\n" \
  "ds_read_b128 v[36:39], v5 offset:5120\n" \
  "ds_read_b128 v[40:43], v5 offset:6144\n" \
  "s_waitcnt lgkmcnt(6)\n" \
  "v_smfmac_f32_16x16x64_bf16 v[44:47], v[16:19], a[0:7], v0\n" \
  "v_smfmac_f32_16x16x64_bf16 v[48:51], v[16:19], a[56:63], v0\n" \
  "v_smfmac_f32_16x16x64_bf16 v[52:55], v[16:19], a[112:119], v0\n" \
  "v_smfmac_f32_16x16x64_bf16 v[56:59], v[16:19], a[168:175], v0\n" \
  "s_waitcnt lgkmcnt(5)\n" \
  "v_smfmac_f32_16x16x64_bf16 v[44:47], v[20:23], a[8:15], v0\n" \
  "v_smfmac_f32_16x16x64_bf16 v[48:51], v[20:23], a[64:71], v0\n" \
  "v_smfmac_f32_16x16x64_bf16 v[52:55], v[20:23], a[120:127], v0\n" \
  "v_smfmac_f32_16x16x64_bf16 v[56:59], v[20:23], a[176:183], v0\n" \
  "s_waitcnt lgkmcnt(4)\n" \
  "v_smfmac_f32_16x16x64_bf16 v[44:47], v[24:27], a[16:23], v0\n" \
  "buffer_load_dwordx4 v[96:99], v1, %[rw], s50 offen offset:0 sc1\n" \
  "v_smfmac_f32_16x16x64_bf16 v[48:51], v[24:27], a[72:79], v0\n" \
  "buffer_load_dwordx4 v[100:103], v1, %[rw], s50 offen offset:1024 sc1\n" \
  "v_smfmac_f32_16x16x64_bf16 v[52:55], v[24:27], a[128:135], v0\n" \
  "buffer_load_dwordx4 v[104:107], v1, %[rw], s50 offen offset:2048 sc1\n" \
  "v_smfmac_f32_16x16x64_bf16 v[56:59], v[24:27], a[184:191], v0\n" \
  "buffer_load_dwordx4 v[108:111], v1, %[rw], s50 offen offset:3072 sc1\n" \
  "s_waitcnt lgkmcnt(3)\n" \
  "v_smfmac_f32_16x16x64_bf16 v[44:47], v[28:31], a[24:31], v0\n" \
  "buffer_load_dwordx4 v[112:115], v1, %[rw], s51 offen offset:0 sc1\n" \
  "v_smfmac_f32_16x16x64_bf16 v[48:51], v[28:31], a[80:87], v0\n" \
  "buffer_load_dwordx4 v[116:119], v1, %[rw], s51 offen offset:1024 sc1\n" \
  "v_smfmac_f32_16x16x64_bf16 v[52:55], v[28:31], a[136:143], v0\n" \
  "buffer_load_dwordx4 v[120:123], v1, %[rw], s51 offen offset:2048 sc1\n" \
  "v_smfmac_f32_16x16x64_bf16 v[56:59], v[28:31], a[192:199], v0\n" \
  "buffer_load_dwordx4 v[124:127], v1, %[rw], s51 offen offset:3072 sc1\n" \
  "s_waitcnt lgkmcnt(2)\n" \
  "v_smfmac_f32_16x16x64_bf16 v[44:47], v[32:35], a[32:39], v0\n" \
  "buffer_load_dword v188, v10, %[rw], s48 offen offset:4 sc1\n" \
  "v_smfmac_f32_16x16x64_bf16 v[48:51], v[32:35], a[88:95], v0\n" \
  "v_smfmac_f32_16x16x64_bf16 v[52:55], v[32:35], a[144:151], v0\n" \
  "v_smfmac_f32_16x16x64_bf16 v[56:59], v[32:35], a[200:207], v0\n" \
  "s_waitcnt lgkmcnt(1)\n" \
  "v_smfmac_f32_16x16x64_bf16 v[44:47], v[36:39], a[40:47], v0\n" \
  "v_smfmac_f32_16x16x64_bf16 v[48:51], v[36:39], a[96:103], v0\n" \
  "v_smfmac_f32_16x16x64_bf16 v[52:55], v[36:39], a[152:159], v0\n" \
  "v_smfmac_f32_16x16x64_bf16 v[56:59], v[36:39], a[208:215], v0\n" \
  "s_waitcnt lgkmcnt(0)\n" \
  "v_smfmac_f32_16x16x64_bf16 v[44:47], v[40:43], a[48:55], v0\n" \
  "v_smfmac_f32_16x16x64_bf16 v[48:51], v[40:43], a[104:111], v0\n" \
  "v_smfmac_f32_16x16x64_bf16 v[52:55], v[40:43], a[160:167], v0\n" \
  "v_smfmac_f32_16x16x64_bf16 v[56:59], v[40:43], a[216:223], v0\n" \
  "s_branch 14f\n" \
  ".p2align 6\n" \
  "11:\n" \
  "s_mov_b32 s45, %[spin]\n" \
  "v_cmp_ne_u32 s[60:61], 0, v15\n" \
  "v_cmp_ne_u32 s[62:63], 1, v15\n" \
  "v_cmp_ne_u32 s[64:65], 2, v15\n" \
  "s_cmp_lg_u32 %[wv], 1\n" \
  "s_cbranch_scc1 43f\n" \
  "41:\n" \
  "s_memtime s[52:53]\n" \
  "s_waitcnt lgkmcnt(0)\n" \
  "s_sub_i32 s56, s50, s52\n" \
  "s_sub_u32 s53, s56, 125\n" \
  "s_cmp_lt_u32 s53, 16384\n" \
  "s_cbranch_scc1 41b\n" \
  "s_max_i32 s56, s56, 0\n" \
  "s_and_b32 s56, s56, 0x7c\n" \
  "s_getpc_b64 s[58:59]\n" \
  "42:\n" \
  "s_sub_u32 s56, 43f-42b, s56\n" \
  "s_add_u32 s58, s58, s56\n" \
  "s_addc_u32 s59, s59, 0\n" \
  "s_setpc_b64 s[58:59]\n" \
  "s_nop 0\n" \
  "s_nop 0\n" \
  "s_nop 0\n" \
  "s_nop 0\n" \
  "s_nop 0\n" \
  "s_nop 0\n" \
  "s_nop 0\n" \
  "s_nop 0\n" \
  "s_nop 0\n" \
  "s_nop 0\n" \
  "s_nop 0\n" \
  "s_nop 0\n" \
  "s_nop 0\n" \
  "s_nop 0\n" \
  "s_nop 0\n" \
  "s_nop 0\n" \
  "s_nop 0\n" \
  "s_nop 0\n" \
  "s_nop 0\n" \
  "s_nop 0\n" \
  "s_nop 0\n" \
  "s_nop 0\n" \
  "s_nop 0\n" \
  "s_nop 0\n" \
  "s_nop 0\n" \
  "s_nop 0\n" \
  "s_nop 0\n" \
  "s_nop 0\n" \
  "s_nop 0\n" \
  "s_nop 0\n" \
  "s_nop 0\n" \
  "43:\n" \
  "s_barrier\n" \
  "s_memtime s[78:79]\n" \
  "2:\n" \
  "s_cmp_lg_u32 %[wv], 3\n" \
  "s_cbranch_scc1 17f\n" \
  "s_mov_b64 exec, s[64:65]\n" \
  "buffer_load_dwordx4 v[24:27], v2, %[rw], s41 offen offset:2048 sc1\n" \
  "17:\n" \
  "s_mov_b64 exec, s[60:61]\n" \
  "buffer_load_dwordx4 v[16:19], v2, %[rw], s41 offen sc1\n" \
  "s_mov_b64 exec, s[62:63]\n" \
  "buffer_load_dwordx4 v[20:23], v2, %[rw], s41 offen offset:1024 sc1\n" \
  "s_not_b64 exec, exec\n" \
  "ds_read_b128 v[20:23], v4 offset:1024\n" \
  "s_not_b64 exec, s[60:61]\n" \
  "ds_read_b128 v[16:19], v4\n" \
  "s_not_b64 exec, s[64:65]\n" \
  "ds_read_b128 v[24:27], v1\n" \
  "s_mov_b64 exec, -1\n" \
  "buffer_load_dword v188, v10, %[rw], s41 offen sc1\n" \
  "s_nop 7\n" \
  "v_mov_b32 v44, 0\n" \
  "v_mov_b32 v45, 0\n" \
  "v_mov_b32 v46, 0\n" \
  "v_mov_b32 v47, 0\n" \
  "v_mov_b32 v48, 0\n" \
  "v_mov_b32 v49, 0\n" \
  "v_mov_b32 v50, 0\n" \
  "v_mov_b32 v51, 0\n" \
  "v_mov_b32 v52, 0\n" \
  "v_mov_b32 v53, 0\n" \
  "v_mov_b32 v54, 0\n" \
  "v_mov_b32 v55, 0\n" \
  "v_mov_b32 v56, 0\n" \
  "v_mov_b32 v57, 0\n" \
  "v_mov_b32 v58, 0\n" \
  "v_mov_b32 v59, 0\n" \
  "s_sub_u32 s45, s45, 1\n" \
  "s_cselect_b64 s[58:59], 0, -1\n" \
  "s_waitcnt vmcnt(2) lgkmcnt(0)\n" \
  "v_smfmac_f32_16x16x64_bf16 v[44:47], v[16:19], a[0:7], v0\n" \
  "ds_write_b128 v4, v[16:19]\n" \
  "v_max3_u32 v184, v16, v17, v18\n" \
  "v_smfmac_f32_16x16x64_bf16 v[48:51], v[16:19], a[56:63], v0\n" \
  "ds_write_b128 v1, v[24:27]\n" \
  "v_max3_u32 v184, v184, v19, v24\n" \
  "v_smfmac_f32_16x16x64_bf16 v[52:55], v[16:19], a[112:119], v0\n" \
  "s_waitcnt vmcnt(1)\n" \
  "ds_write_b128 v4, v[20:23] offset:1024\n" \
  "v_max3_u32 v184, v184, v25, v26\n" \
  "v_smfmac_f32_16x16x64_bf16 v[56:59], v[16:19], a[168:175], v0\n" \
  "v_max3_u32 v184, v184, v27, v20\n" \
  "v_max3_u32 v184, v184, v21, v22\n" \
  "v_smfmac_f32_16x16x64_bf16 v[44:47], v[20:23], a[8:15], v0\n" \
  "v_max_u32 v184, v184, v23\n" \
  "v_cmp_eq_u32 vcc, -1, v184\n" \
  "v_smfmac_f32_16x16x64_bf16 v[48:51], v[20:23], a[64:71], v0\n" \
  "s_and_b64 vcc, vcc, s[58:59]\n" \
  "s_cbranch_vccnz 2b\n" \
  "s_waitcnt lgkmcnt(0)\n" \
  "s_barrier\n" \
  "ds_read_b128 v[24:27], v5\n" \
  "ds_read_b128 v[28:31], v13\n" \
  "ds_read_b128 v[32:35], v13 offset:1024\n" \
  "ds_read_b128 v[36:39], v3\n" \
  "ds_read_b128 v[40:43], v3 offset:1024\n" \
  "v_smfmac_f32_16x16x64_bf16 v[52:55], v[20:23], a[120:127], v0\n" \
  "s_sub_u32 s50, s78, s50\n" \
  "s_cmp_gt_i32 s50, %[fth]\n" \
  "s_cselect_b32 s50, %[kup], %[kdn]\n" \
  "s_add_i32 s51, s51, s50\n" \
  "v_smfmac_f32_16x16x64_bf16 v[56:59], v[20:23], a[176:183], v0\n" \
  "s_waitcnt lgkmcnt(4)\n" \
  "v_smfmac_f32_16x16x64_bf16 v[44:47], v[24:27], a[16:23], v0\n" \
  "v_smfmac_f32_16x16x64_bf16 v[48:51], v[24:27], a[72:79], v0\n" \
  "v_smfmac_f32_16x16x64_bf16 v[52:55], v[24:27], a[128:135], v0\n" \
  "v_smfmac_f32_16x16x64_bf16 v[56:59], v[24:27], a[184:191], v0\n" \
  "s_waitcnt lgkmcnt(3)\n" \
  "v_smfmac_f32_16x16x64_bf16 v[44:47], v[28:31], a[24:31], v0\n" \
  "buffer_load_dword v185, v9, %[rw], s42 offen sc1\n" \
  "v_smfmac_f32_16x16x64_bf16 v[48:51], v[28:31], a[80:87], v0\n" \
  "v_smfmac_f32_16x16x64_bf16 v[52:55], v[28:31], a[136:143], v0\n" \
  "v_smfmac_f32_16x16x64_bf16 v[56:59], v[28:31], a[192:199], v0\n" \
  "s_waitcnt lgkmcnt(2)\n" \
  "v_smfmac_f32_16x16x64_bf16 v[44:47], v[32:35], a[32:39], v0\n" \
  "v_smfmac_f32_16x16x64_bf16 v[48:51], v[32:35], a[88:95], v0\n" \
  "v_smfmac_f32_16x16x64_bf16 v[52:55], v[32:35], a[144:151], v0\n" \
  "v_smfmac_f32_16x16x64_bf16 v[56:59], v[32:35], a[200:207], v0\n" \
  "s_waitcnt lgkmcnt(1)\n" \
  "v_smfmac_f32_16x16x64_bf16 v[44:47], v[36:39], a[40:47], v0\n" \
  "v_smfmac_f32_16x16x64_bf16 v[48:51], v[36:39], a[96:103], v0\n" \
  "v_smfmac_f32_16x16x64_bf16 v[52:55], v[36:39], a[152:159], v0\n" \
  "s_waitcnt vmcnt(1)\n" \
  "v_max_i32_dpp v188, v188, v188 row_ror:8 row_mask:0xf bank_mask:0xf\n" \
  "v_smfmac_f32_16x16x64_bf16 v[56:59], v[36:39], a[208:215], v0\n" \
  "v_max_i32_dpp v188, v188, v188 row_ror:4 row_mask:0xf bank_mask:0xf\n" \
  "s_waitcnt lgkmcnt(0)\n" \
  "v_smfmac_f32_16x16x64_bf16 v[44:47], v[40:43], a[48:55], v0\n" \
  "v_max_i32_dpp v188, v188, v188 row_ror:2 row_mask:0xf bank_mask:0xf\n" \
  "v_smfmac_f32_16x16x64_bf16 v[48:51], v[40:43], a[104:111], v0\n" \
  "v_max_i32_dpp v188, v188, v188 row_ror:1 row_mask:0xf bank_mask:0xf\n" \
  "v_smfmac_f32_16x16x64_bf16 v[52:55], v[40:43], a[160:167], v0\n" \
  "v_readlane_b32 s49, v188, 0\n" \
  "v_smfmac_f32_16x16x64_bf16 v[56:59], v[40:43], a[216:223], v0\n" \
  "v_readlane_b32 s50, v188, 16\n" \
  "14:\n" \
  "v_add_f32 v44, v44, v45\n" \
  "v_add_f32 v45, v46, v47\n" \
  "v_cvt_pk_bf16_f32 v128, v44, v45\n" \
  "v_add_f32 v48, v48, v49\n" \
  "v_add_f32 v49, v50, v51\n" \
  "v_cvt_pk_bf16_f32 v129, v48, v49\n" \
  "v_add_f32 v52, v52, v53\n" \
  "v_add_f32 v53, v54, v55\n" \
  "v_cvt_pk_bf16_f32 v130, v52, v53\n" \
  "s_nop 0\n" \
  "v_add_f32 v56, v56, v57\n" \
  "v_add_f32 v57, v58, v59\n" \
  "v_cvt_pk_bf16_f32 v131, v56, v57\n" \
  "ds_write_b64 v6, v[128:129]\n" \
  "ds_write_b64 v6, v[130:131] offset:512\n" \
  "s_cmp_lg_u32 %[wv], 0\n" \
  "s_cbranch_scc1 12f\n" \
  "s_add_u32 s49, s42, 4096\n" \
  "s_add_u32 s50, s42, 16384\n" \
  "s_add_u32 s51, s42, 20480\n" \
  "s_add_u32 s41, s41, 16384\n" \
  "s_mov_b32 s45, %[spin]\n" \
  ".p2align 6\n" \
  "s_waitcnt lgkmcnt(0)\n" \
  "s_barrier\n" \
  "6:\n" \
  "buffer_load_dwordx4 v[144:147], v8, %[rw], s42 offen offset:0 sc1\n" \
  "buffer_load_dwordx4 v[148:151], v8, %[rw], s42 offen offset:1024 sc1\n" \
  "buffer_load_dwordx4 v[152:155], v8, %[rw], s42 offen offset:2048 sc1\n" \
  "buffer_load_dwordx4 v[156:159], v8, %[rw], s42 offen offset:3072 sc1\n" \
  "buffer_load_dwordx4 v[160:163], v8, %[rw], s50 offen offset:0 sc1\n" \
  "buffer_load_dwordx4 v[164:167], v8, %[rw], s50 offen offset:1024 sc1\n" \
  "buffer_load_dwordx4 v[168:171], v8, %[rw], s50 offen offset:2048 sc1\n" \
  "buffer_load_dwordx4 v[172:175], v8, %[rw], s50 offen offset:3072 sc1\n" \
  "buffer_load_dwordx4 v[176:179], v1, %[rw], s49 offen offset:0 sc1\n" \
  "buffer_load_dwordx4 v[180:183], v1, %[rw], s51 offen offset:0 sc1\n" \
  "ds_read_b128 v[128:131], v13 offset:0\n" \
  "ds_read_b128 v[132:135], v13 offset:1024\n" \
  "ds_read_b128 v[136:139], v13 offset:2048\n" \
  "ds_read_b128 v[140:143], v13 offset:3072\n" \
  "v_mov_b32 v60, 0\n" \
  "v_mov_b32 v61, 0\n" \
  "v_mov_b32 v62, 0\n" \
  "v_mov_b32 v63, 0\n" \
  "v_bfrev_b32 v184, 1\n" \
  "s_sub_u32 s45, s45, 1\n" \
  "s_waitcnt lgkmcnt(0)\n" \
  "s_waitcnt vmcnt(9)\n" \
  "v_smfmac_f32_16x16x64_bf16 v[60:63], v[144:147], v[64:71], v12\n" \
  "s_waitcnt vmcnt(8)\n" \
  "v_smfmac_f32_16x16x64_bf16 v[60:63], v[148:151], v[72:79], v12\n" \
  "s_waitcnt vmcnt(7)\n" \
  "v_smfmac_f32_16x16x64_bf16 v[60:63], v[152:155], v[80:87], v12\n" \
  "s_waitcnt vmcnt(6)\n" \
  "v_smfmac_f32_16x16x64_bf16 v[60:63], v[156:159], v[88:95], v12\n" \
  "s_waitcnt vmcnt(5)\n" \
  "v_smfmac_f32_16x16x64_bf16 v[60:63], v[160:163], v[96:103], v12\n" \
  "s_waitcnt vmcnt(4)\n" \
  "v_smfmac_f32_16x16x64_bf16 v[60:63], v[164:167], v[104:111], v12\n" \
  "s_waitcnt vmcnt(3)\n" \
  "v_smfmac_f32_16x16x64_bf16 v[60:63], v[168:171], v[112:119], v12\n" \
  "s_waitcnt vmcnt(2)\n" \
  "v_smfmac_f32_16x16x64_bf16 v[60:63], v[172:175], v[120:127], v12\n" \
  "s_waitcnt vmcnt(1)\n" \
  "v_smfmac_f32_16x16x64_bf16 v[60:63], v[176:179], v[128:135], v12\n" \
  "s_waitcnt vmcnt(0)\n" \
  "v_smfmac_f32_16x16x64_bf16 v[60:63], v[180:183], v[136:143], v12\n" \
  "s_nop 7\n" \
  "v_pk_add_f32 v[60:61], v[60:61], v[62:63]\n" \
  "v_cvt_pk_bf16_f32 v62, v60, v61\n" \
  "v_cmp_u_f32 vcc, v60, v61\n" \
  "s_nop 0\n" \
  "v_mov_b32_dpp v63, v62 row_ror:8 row_mask:0xf bank_mask:0xf\n" \
  "v_cndmask_b32 v184, v7, v184, vcc\n" \
  "v_perm_b32 v62, v63, v62, v11\n" \
  "buffer_store_dword v62, v184, %[rw], s41 offen sc1\n" \
  "s_cbranch_vccnz 15f\n" \
  "7:\n" \
  "ds_write_b32 v3, v62\n" \
  "s_sub_u32 s41, s41, 16384\n" \
  "9:\n" \
  "s_add_u32 s41, s41, 16384\n" \
  "s_add_u32 s42, s42, 32768\n" \
  "s_add_u32 s43, s43, 458752\n" \
  "s_sub_u32 s49, s43, %[sy]\n" \
  "s_cmp_eq_u32 s49, %[yr]\n" \
  "s_cselect_b32 s43, %[sy], s43\n" \
  "s_add_u32 s48, s48, 4\n" \
  "s_sub_u32 s44, s44, 1\n" \
  "s_cmp_lg_u32 s44, 0\n" \
  "s_cbranch_scc1 1b\n" \
  "s_waitcnt vmcnt(0)\n" \
  "s_endpgm\n" \
  "15:\n" \
  "v_max3_u32 v184, v144, v145, v146\n" \
  "v_max_u32 v184, v184, v147\n" \
  "v_max3_u32 v184, v184, v148, v149\n" \
  "v_max3_u32 v184, v184, v150, v151\n" \
  "v_max3_u32 v184, v184, v152, v153\n" \
  "v_max3_u32 v184, v184, v154, v155\n" \
  "v_max3_u32 v184, v184, v156, v157\n" \
  "v_max3_u32 v184, v184, v158, v159\n" \
  "v_max3_u32 v184, v184, v160, v161\n" \
  "v_max3_u32 v184, v184, v162, v163\n" \
  "v_max3_u32 v184, v184, v164, v165\n" \
  "v_max3_u32 v184, v184, v166, v167\n" \
  "v_max3_u32 v184, v184, v168, v169\n" \
  "v_max3_u32 v184, v184, v170, v171\n" \
  "v_max3_u32 v184, v184, v172, v173\n" \
  "v_max3_u32 v184, v184, v174, v175\n" \
  "v_max3_u32 v184, v184, v176, v177\n" \
  "v_max3_u32 v184, v184, v178, v179\n" \
  "v_max3_u32 v184, v184, v180, v181\n" \
  "v_max3_u32 v184, v184, v182, v183\n" \
  "v_cmp_eq_u32 vcc, -1, v184\n" \
  "s_cmp_gt_i32 s45, 0\n" \
  "s_cbranch_scc0 16f\n" \
  "s_cbranch_vccnz 6b\n" \
  "16:\n" \
  "v_min_u32 v62, -2, v62\n" \
  "buffer_store_dword v62, v7, %[rw], s41 offen sc1\n" \
  "s_branch 7b\n" \
  ".p2align 6\n" \
  "12:\n" \
  "s_cmp_eq_u32 %[wv], 1\n" \
  "s_cbranch_scc1 13f\n" \
  "s_waitcnt lgkmcnt(0)\n" \
  "s_barrier\n" \
  "s_branch 9b\n" \
  "13:\n" \
  "s_max_i32 s49, s49, s50\n" \
  "s_add_i32 s49, s49, %[kdn]\n" \
  "s_max_i32 s51, s51, s49\n" \
  "s_mov_b32 s45, %[spin]-1\n" \
  "s_waitcnt vmcnt(0)\n" \
  "s_branch 34f\n" \
  "30:\n" \
  "buffer_load_dword v185, v9, %[rw], s42 offen sc1\n" \
  "s_sub_u32 s45, s45, 1\n" \
  "s_waitcnt vmcnt(0)\n" \
  "s_cbranch_scc1 37f\n" \
  "34:\n" \
  "v_cmp_eq_u32 vcc, -1, v185\n" \
  "s_cbranch_vccnz 30b\n" \
  "37:\n" \
  "v_subrev_u32 v185, s81, v185\n" \
  "s_nop 1\n" \
  "v_max_i32_dpp v185, v185, v185 row_ror:8 row_mask:0xf bank_mask:0xf\n" \
  "s_nop 1\n" \
  "v_max_i32_dpp v185, v185, v185 row_ror:4 row_mask:0xf bank_mask:0xf\n" \
  "s_nop 1\n" \
  "v_max_i32_dpp v185, v185, v185 row_ror:2 row_mask:0xf bank_mask:0xf\n" \
  "s_nop 1\n" \
  "v_max_i32_dpp v185, v185, v185 row_ror:1 row_mask:0xf bank_mask:0xf\n" \
  "s_add_u32 s56, s80, s81\n" \
  "s_nop 0\n" \
  "v_readfirstlane_b32 s55, v185\n" \
  "v_readlane_b32 s50, v185, 16\n" \
  "s_add_i32 s55, s55, s51\n" \
  "s_sub_i32 s55, s55, s46\n" \
  "s_min_i32 s50, s50, 9000\n" \
  "s_add_u32 s50, s50, s56\n" \
  "s_min_i32 s55, s55, 8000\n" \
  "s_add_u32 s55, s55, s56\n" \
  "31:\n" \
  "s_memtime s[52:53]\n" \
  "s_waitcnt lgkmcnt(0)\n" \
  "s_sub_i32 s56, s55, s52\n" \
  "s_sub_u32 s53, s56, 125\n" \
  "s_cmp_lt_u32 s53, 16384\n" \
  "s_cbranch_scc1 31b\n" \
  "s_max_i32 s56, s56, 0\n" \
  "s_and_b32 s56, s56, 0x7c\n" \
  "s_getpc_b64 s[58:59]\n" \
  "32:\n" \
  "s_sub_u32 s56, 33f-32b, s56\n" \
  "s_add_u32 s58, s58, s56\n" \
  "s_addc_u32 s59, s59, 0\n" \
  "s_setpc_b64 s[58:59]\n" \
  "s_nop 0\n" \
  "s_nop 0\n" \
  "s_nop 0\n" \
  "s_nop 0\n" \
  "s_nop 0\n" \
  "s_nop 0\n" \
  "s_nop 0\n" \
  "s_nop 0\n" \
  "s_nop 0\n" \
  "s_nop 0\n" \
  "s_nop 0\n" \
  "s_nop 0\n" \
  "s_nop 0\n" \
  "s_nop 0\n" \
  "s_nop 0\n" \
  "s_nop 0\n" \
  "s_nop 0\n" \
  "s_nop 0\n" \
  "s_nop 0\n" \
  "s_nop 0\n" \
  "s_nop 0\n" \
  "s_nop 0\n" \
  "s_nop 0\n" \
  "s_nop 0\n" \
  "s_nop 0\n" \
  "s_nop 0\n" \
  "s_nop 0\n" \
  "s_nop 0\n" \
  "s_nop 0\n" \
  "s_nop 0\n" \
  "s_nop 0\n" \
  "33:\n" \
  "s_barrier\n" \
  "s_add_u32 s50, s50, %[slp1]\n" \
  "s_cmp_lg_u32 s45, %[spin]-1\n" \
  "s_cselect_b32 s49, 160, 0\n" \
  "s_add_u32 s50, s50, s49\n" \
  "s_mov_b32 s40, s80\n" \
  "s_memtime s[52:53]\n" \
  "s_nop 0\n" \
  "s_memrealtime s[54:55]\n" \
  "s_nop 0\n" \
  "s_memrealtime s[56:57]\n" \
  "s_nop 0\n" \
  "s_memrealtime s[58:59]\n" \
  "s_nop 0\n" \
  "s_memrealtime s[60:61]\n" \
  "s_nop 0\n" \
  "s_memrealtime s[62:63]\n" \
  "s_nop 0\n" \
  "s_memrealtime s[64:65]\n" \
  "s_nop 0\n" \
  "s_memrealtime s[66:67]\n" \
  "s_nop 0\n" \
  "s_memrealtime s[68:69]\n" \
  "s_nop 0\n" \
  "s_memrealtime s[70:71]\n" \
  "s_nop 0\n" \
  "s_memrealtime s[72:73]\n" \
  "s_nop 0\n" \
  "s_memrealtime s[74:75]\n" \
  "s_nop 0\n" \
  "s_memrealtime s[76:77]\n" \
  "s_waitcnt lgkmcnt(0)\n" \
  "s_add_u32 s54, s54, s56\n" \
  "s_add_u32 s54, s54, s58\n" \
  "s_add_u32 s54, s54, s60\n" \
  "s_add_u32 s54, s54, s62\n" \
  "s_add_u32 s54, s54, s64\n" \
  "s_add_u32 s54, s54, s66\n" \
  "s_add_u32 s54, s54, s68\n" \
  "s_add_u32 s54, s54, s70\n" \
  "s_add_u32 s54, s54, s72\n" \
  "s_add_u32 s54, s54, s74\n" \
  "s_add_u32 s54, s54, s76\n" \
  "s_lshl_b32 s81, s54, 1\n" \
  "s_sub_u32 s80, s52, s81\n" \
  "s_cmp_eq_u32 s44, 63\n" \
  "s_cbranch_scc1 8f\n" \
  "s_sub_u32 s57, s40, s80\n" \
  "s_sub_i32 s57, s57, s46\n" \
  "s_add_i32 s57, s57, 2\n" \
  "s_ashr_i32 s57, s57, 2\n" \
  "s_max_i32 s57, s57, -16\n" \
  "s_min_i32 s57, s57, 16\n" \
  "s_add_i32 s46, s46, s57\n" \
  "8:\n" \
  "s_branch 9b\n" \
  ".Lcode_z:\n"
#define ASM_CLOB "v0", "v1", "v2", "v3", "v4", "v5", "v6", "v7", "v8", "v9", "v10", "v11", "v12", "v13", "v14", "v15", "v16", "v17", "v18", "v19", "v20", "v21", "v22", "v23", "v24", "v25", "v26", "v27", "v28", "v29", "v30", "v31", "v32", "v33", "v34", "v35", "v36", "v37", "v38", "v39", "v40", "v41", "v42", "v43", "v44", "v45", "v46", "v47", "v48", "v49", "v50", "v51", "v52", "v53", "v54", "v55", "v56", "v57", "v58", "v59", "v60", "v61", "v62", "v63", "v64", "v65", "v66", "v67", "v68", "v69", "v70", "v71", "v72", "v73", "v74", "v75", "v76", "v77", "v78", "v79", "v80", "v81", "v82", "v83", "v84", "v85", "v86", "v87", "v88", "v89", "v90", "v91", "v92", "v93", "v94", "v95", "v96", "v97", "v98", "v99", "v100", "v101", "v102", "v103", "v104", "v105", "v106", "v107", "v108", "v109", "v110", "v111", "v112", "v113", "v114", "v115", "v116", "v117", "v118", "v119", "v120", "v121", "v122", "v123", "v124", "v125", "v126", "v127", "v128", "v129", "v130", "v131", "v132", "v133", "v134", "v135", "v136", "v137", "v138", "v139", "v140", "v141", "v142", "v143", "v144", "v145", "v146", "v147", "v148", "v149", "v150", "v151", "v152", "v153", "v154", "v155", "v156", "v157", "v158", "v159", "v160", "v161", "v162", "v163", "v164", "v165", "v166", "v167", "v168", "v169", "v170", "v171", "v172", "v173", "v174", "v175", "v176", "v177", "v178", "v179", "v180", "v181", "v182", "v183", "v184", "v185", "v186", "v187", "v188", "v189", "v190", "v191", "v192", "v193", "v194", "v195", "v196", "v197", "v198", "v199", "v200", "v201", "v202", "v203", "v204", "v205", "v206", "v207", "v208", "v209", "v210", "v211", "v212", "v213", "v214", "v215", "v216", "v217", "v218", "v219", "v220", "v221", "v222", "v223", "v224", "v225", "v226", "v227", "v228", "v229", "v230", "v231", "v232", "v233", "v234", "v235", "v236", "v237", "v238", "v239", "v240", "v241", "v242", "v243", "v244", "v245", "v246", "v247", "v248", "v249", "v250", "v251", "v252", "v253", "v254", "v255", "a0", "a1", "a2", "a3", "a4", "a5", "a6", "a7", "a8", "a9", "a10", "a11", "a12", "a13", "a14", "a15", "a16", "a17", "a18", "a19", "a20", "a21", "a22", "a23", "a24", "a25", "a26", "a27", "a28", "a29", "a30", "a31", "a32", "a33", "a34", "a35", "a36", "a37", "a38", "a39", "a40", "a41", "a42", "a43", "a44", "a45", "a46", "a47", "a48", "a49", "a50", "a51", "a52", "a53", "a54", "a55", "a56", "a57", "a58", "a59", "a60", "a61", "a62", "a63", "a64", "a65", "a66", "a67", "a68", "a69", "a70", "a71", "a72", "a73", "a74", "a75", "a76", "a77", "a78", "a79", "a80", "a81", "a82", "a83", "a84", "a85", "a86", "a87", "a88", "a89", "a90", "a91", "a92", "a93", "a94", "a95", "a96", "a97", "a98", "a99", "a100", "a101", "a102", "a103", "a104", "a105", "a106", "a107", "a108", "a109", "a110", "a111", "a112", "a113", "a114", "a115", "a116", "a117", "a118", "a119", "a120", "a121", "a122", "a123", "a124", "a125", "a126", "a127", "a128", "a129", "a130", "a131", "a132", "a133", "a134", "a135", "a136", "a137", "a138", "a139", "a140", "a141", "a142", "a143", "a144", "a145", "a146", "a147", "a148", "a149", "a150", "a151", "a152", "a153", "a154", "a155", "a156", "a157", "a158", "a159", "a160", "a161", "a162", "a163", "a164", "a165", "a166", "a167", "a168", "a169", "a170", "a171", "a172", "a173", "a174", "a175", "a176", "a177", "a178", "a179", "a180", "a181", "a182", "a183", "a184", "a185", "a186", "a187", "a188", "a189", "a190", "a191", "a192", "a193", "a194", "a195", "a196", "a197", "a198", "a199", "a200", "a201", "a202", "a203", "a204", "a205", "a206", "a207", "a208", "a209", "a210", "a211", "a212", "a213", "a214", "a215", "a216", "a217", "a218", "a219", "a220", "a221", "a222", "a223", "a224", "a225", "a226", "a227", "a228", "a229", "a230", "a231", "a232", "a233", "a234", "a235", "a236", "a237", "a238", "a239", "a240", "a241", "a242", "a243", "a244", "a245", "a246", "a247", "a248", "a249", "a250", "a251", "a252", "a253", "a254", "a255", "s40", "s41", "s42", "s43", "s44", "s45", "s46", "s47", "s48", "s49", "s50", "s51", "s52", "s53", "s54", "s55", "s56", "s57", "s58", "s59", "s60", "s61", "s62", "s63", "s64", "s65", "s66", "s67", "s68", "s69", "s70", "s71", "s72", "s73", "s74", "s75", "s76", "s77", "s78", "s79", "s80", "s81", "s82", "s83", "s84", "s85", "s86", "s87", "vcc", "scc", "m0", "memory"

#define SLP_S 8
#define SLP_V1 12
#define SLP_V2 4

DEVI u32 pslot(int s) { return PH2 == 16384 ? (u32)s * 32768u : (u32)(s >> 1) * 65536u + (u32)(s & 1) * 16384u; }
DEVI u32 tail_time(const Ctx& c, int h, int s, u32 pw) {
  const int l = lane_id();
  u32 m = 0;
  for (int sp = 0; sp < SPIN_MAX; ++sp) {
    cbar();
    m = __builtin_amdgcn_raw_buffer_load_b32(c.rw, l < 3 ? XFLAG + 8u + pslot(s - 3 + l) : 0x80000000u, OFF_P + ((pw >> 1) & 8192u), SC1);
    if (!any_sent(m)) break;
    __builtin_amdgcn_s_sleep(1);
  }
  const u32 t1 = __builtin_amdgcn_readlane(m, 1), t2 = __builtin_amdgcn_readlane(m, 2);
  return t2 + (min(min(t2 - t1, t1 - uni(m)), 4096u) * 174u >> 8);
}
DEVI void tail_wait(u32 t) {
  for (int sp = 0; sp < 1024; ++sp)
    if (t - (u32)__builtin_amdgcn_s_memrealtime() * 24u > 8192u) break;
  cbar();
}

DEVI void role_S(const Ctx& c, int h) {
  const int l = lane_id(), w = c.w;
  char* L = c.lds;
  const u32 LB = lds_addr(L);
  constexpr int L_X = 0, L_MEM = 28672, L_SC = L_MEM + 8192;
  const int pa = w == 0 ? 1 : (w == 3 ? 1 : 0), pb = w == 2 ? 1 : 2, sg = w == 3 ? 1 : 6;
  const u32 kbs = (u32)(2 * w) | (u32)(w == 3 ? 0 : 2 * w + 1) << 4 | (u32)(2 * pa) << 8 | (u32)(2 * pa + 1) << 12 | (u32)(2 * pb) << 16 | (u32)(2 * pb + 1) << 20 | (u32)sg << 24;
  *(f2*)(L + L_SC + (w * 328 + 320) * 4) = (f2){-INFINITY, -INFINITY};
  wait_pieces(c, (u32)(h * 8 + l * 4 + w) * 128u, l < 2);
  asm volatile("s_waitcnt lgkmcnt(0)\n" ASM_S
               :: [rw] "s"(c.rw), [rx] "s"(c.rx), [cst] "s"(uni(LB + L_CST + w * 6144)), [kbs] "s"(uni(kbs)),
                  [wb] "s"(uni(OFF_WA + (u32)(h * 4 + w) * 100352u)), [sm] "n"(OFF_MEMA), [sp] "n"(OFF_P), [pw] "n"(OFF_PW), [e0] "s"(c.e0),
                  [xm0] "s"(uni(LB + L_X + w * 7168)), [dbo] "s"(uni(w == 0 ? (u32)((h >> 3) * PH2 + 5120 + (h & 7) * 128) : (w == 1 ? XFLAG : 0x80000000u))),
                  [slp] "s"(c.d0), [fth] "n"(FTH_S), KOPS(2), [spin] "n"(SPIN_MAX),
                  [rtv] "s"(uni(RTV_S)), [cnt] "s"(uni(OFF_DBG + 81920u + (u32)(h * 4 + w) * 8u))
               : ASM_CLOB);
  __builtin_amdgcn_endpgm();
}

struct PFrag { int ph[3]; u32 pin[3]; };
DEVI PFrag pfrag(int w, int n, int q) {
  PFrag f;
  f.ph[0] = 4 * w + (n >> 3); f.ph[1] = f.ph[0] + 2; f.ph[2] = 4 * w + 2 * (q >> 1) + (n >> 3);
  f.pin[0] = f.pin[1] = (n & 7) * 16 + q * 128; f.pin[2] = (n & 7) * 16 + 512;
  return f;
}

DEVI void role_V(const Ctx& c, int j) {
  const int l = lane_id(), w = c.w;
  char* L = c.lds;
  const u32 LB = lds_addr(L);
  wait_pieces(c, (u32)((16 + 4 * w) * 8 + l * 4 + j / 7) * 128u, l < 8);
  asm volatile("s_waitcnt lgkmcnt(0)\n" ASM_V
               :: [rw] "s"(c.rw), [cst] "s"(uni(LB + L_CST + w * 6144)), [wb] "s"(uni(OFF_WB + (u32)(j * 16 + 4 * w) * 14336u)),
                  [sm] "n"(OFF_MEMA), [sp] "n"(OFF_P), [pw] "n"(OFF_PW), [e0] "s"(c.e0), [sy] "s"(uni(YBASE)), [yr] "n"(YR * 28 * 16384),
                  [sf] "s"(uni(OFF_YF + (u32)j * 1024u)), [wv] "s"(w),
                  [slp1] "s"(c.d1), [slp2] "s"(c.d2), [fth] "n"(FTH_V), KOPS(1), [spin] "n"(SPIN_MAX),
                  [rtv] "s"(uni(RTV_V)), [cnt] "s"(uni(OFF_DBG + 81920u + 512u + (u32)(j * 4 + w) * 8u))
               : ASM_CLOB);
  __builtin_amdgcn_endpgm();
}

constexpr int NO = 3, NX = 3;
#define XDIR 60
#define ODIR 62
#define XPOLL 8
#define OPOLL 8
DEVI void role_O(const Ctx& c, int j, int set) {
  const int l = lane_id(), w = c.w, n = l & 15, q = l >> 4;
  char* L = c.lds;
  constexpr int L_X = 0, L_RO = 28672;
  u8v W[4][7];
  const int idx = n < 8 ? 0x4444 : 0xEEEE;
  u32 xoff[7];
#pragma unroll
  for (int i = 0; i < 7; ++i) { int f = 7 * w + i, mt = f >= 14, ks = f - 14 * mt; xoff[i] = (u32)(16 * mt + n) * 896u + ks * 64 + q * 16; }
  u4 xr[7];
#pragma unroll
  for (int i = 0; i < 7; ++i) xr[i] = ld16(c.rx, xoff[i], (u32)set * 32u * 896u);
  wait_pieces(c, (u32)((16 + 4 * w) * 8 + l * 4 + j / 7) * 128u, l < 8);
  for (int t = 0; t < set * ODEL; ++t) __builtin_amdgcn_s_sleep(8);
#pragma unroll
  for (int kb = 0; kb < 7; ++kb)
#pragma unroll
    for (int hh = 0; hh < 4; ++hh) {
      u32 so = OFF_WB + (u32)((j * 16 + 4 * w + hh) * 14 + 2 * kb) * 1024u;
      W[hh][kb] = cat(ld16w(c.rw, l * 16, so), ld16w(c.rw, l * 16 + 1024, so));
    }
  cbar();
#pragma unroll
  for (int i = 0; i < 7; ++i) *(u4*)(L + L_X + (7 * w + i) * 1024 + l * 16) = xr[i];
  lds_bar();
  PFrag f = pfrag(w, n, q);
  u32 pxo[3];
#pragma unroll
  for (int i = 0; i < 3; ++i) pxo[i] = f.ph[i] * 2560 + f.pin[i];
  const u32 wr = ((2 * w + (q >> 1)) * 8 + 4 * (q & 1)) * 80 + (4 * (n & 3) + 2 * ((n >> 2) & 1) + (n >> 3)) * 4;
  const u32 ao = q * 256 + (n & 7) * 32 + (n >> 3) * 16;
  const u32 rdo = L_RO + w * 5120 + (l >> 3) * 80 + (l & 7) * 8;
  const u32 sto = (u32)(8 * w + (l >> 3)) * 1792u + 896 + j * 32 + (l & 7) * 4;
  u8v Y[2], Y2[2], Yn[2];
  const f4 z = {0, 0, 0, 0};
  auto values = [&](int s) {
      if (s + NO < NSEG) {
#pragma unroll
        for (int i = 0; i < 7; ++i) xr[i] = ld16(c.rx, xoff[i], (u32)(s + NO) * 32u * 896u);
      }
      f4 acc[4][2];
#pragma unroll
      for (int hh = 0; hh < 4; ++hh) acc[hh][0] = acc[hh][1] = z;
      u4 a0 = *(u4*)(L + L_X + l * 16), a1 = *(u4*)(L + L_X + 14 * 1024 + l * 16);
#pragma unroll
      for (int ks = 0; ks < 14; ++ks) {
        u4 n0 = a0, n1 = a1;
        if (ks < 13) { n0 = *(u4*)(L + L_X + (ks + 1) * 1024 + l * 16); n1 = *(u4*)(L + L_X + (15 + ks) * 1024 + l * 16); }
        __builtin_amdgcn_sched_barrier(0);
#pragma unroll
        for (int hh = 0; hh < 4; ++hh) {
          u4 b = (ks & 1) ? hi4(W[hh][ks >> 1]) : lo4(W[hh][ks >> 1]);
          acc[hh][0] = mfma(a0, b, acc[hh][0]);
          acc[hh][1] = mfma(a1, b, acc[hh][1]);
        }
        __builtin_amdgcn_sched_barrier(0);
        a0 = n0; a1 = n1;
      }
#pragma unroll
      for (int p = 0; p < 2; ++p)
#pragma unroll
        for (int kt = 0; kt < 2; ++kt) {
          Yn[p][4 * kt + 0] = pk(acc[2 * p][kt][0], acc[2 * p][kt][1]);
          Yn[p][4 * kt + 1] = pk(acc[2 * p + 1][kt][0], acc[2 * p + 1][kt][1]);
          Yn[p][4 * kt + 2] = pk(acc[2 * p][kt][2], acc[2 * p][kt][3]);
          Yn[p][4 * kt + 3] = pk(acc[2 * p + 1][kt][2], acc[2 * p + 1][kt][3]);
        }
      const u32 so = OFF_Y + (u32)(((s & (YR - 1)) * 28 + j) * 4 + w) * 4096u;
#pragma unroll
      for (int p = 0; p < 4; ++p) {
        const auto a = __builtin_amdgcn_permlane32_swap(Yn[p >> 1][4 * (p & 1)], Yn[p >> 1][4 * (p & 1) + 1], false, false);
        const auto b = __builtin_amdgcn_permlane32_swap(Yn[p >> 1][4 * (p & 1) + 2], Yn[p >> 1][4 * (p & 1) + 3], false, false);
        st16c(c.rw, l * 16 + p * 1024, so, (u4){a[0], a[1], b[0], b[1]});
      }
  };
  auto ready = [&](int s) {
    asm volatile("s_waitcnt vmcnt(0)" ::: "memory");
    st4c(c.rw, l == 0 ? (u32)s * 4u : 0x80000000u, OFF_YF + (u32)(j * 4 + w) * 256u, 1u);
  };
  values(set);
  RTO(6);
  const u32 parM = wait_go(c);
  RTO(7);
  ready(set);
  for (int s = set; s < NSEG + 2 * NO; s += NO) {
    if (s < NSEG && s >= NO) { values(s); ready(s); }
    lds_bar();
    if (s + NO < NSEG) {
#pragma unroll
      for (int i = 0; i < 7; ++i) *(u4*)(L + L_X + (7 * w + i) * 1024 + l * 16) = xr[i];
    }
    if (s >= 2 * NO) {
      const int t = s - 2 * NO;
      u4 px[12], A[7];
      const u32 so = OFF_PX + (u32)t * 40960u;
      const bool last = t >= ODIR;
      u32 tt = 0;
      if (last) { tt = tail_time(c, (j + 4 * w) & 15, t, parM); tail_wait(tt + c.xt - 1024u); }
      for (int sp = 0; sp < SPIN_MAX && !last; ++sp) {
        cbar();
        u32 m = __builtin_amdgcn_raw_buffer_load_b32(c.rw, l == 0 ? (u32)w * 10240u : 0x80000000u, so, SC1);
        if (!any_sent(m)) break;
        __builtin_amdgcn_s_sleep(OPOLL);
      }
      const u32 sm = OFF_MEMA + (u32)t * 16384u + (parM & 8192u);
      for (int sp = 0; sp < SPIN_MAX; ++sp) {
        cbar();
        u32 m = 0;
#pragma unroll
        for (int i = 0; i < 7; ++i) A[i] = ld16c(c.rw, i * 1024 + ao, sm);
#pragma unroll
        for (int i = 0; i < 12; ++i) px[i] = (u4){0, 0, 0, 0};
        if (!last) {
#pragma unroll
          for (int i = 0; i < 12; ++i) px[i] = ld16c(c.rw, pxo[i % 3] + (i / 3) * 640, so);
#pragma unroll
          for (int i = 0; i < 12; ++i) m = umax3(umax3(m, px[i][0], px[i][1]), px[i][2], px[i][3]);
        }
#pragma unroll
        for (int i = 0; i < 7; ++i) m = umax3(umax3(m, A[i][0], A[i][1]), A[i][2], A[i][3]);
        if (!any_sent(m)) break;
        __builtin_amdgcn_s_sleep(2);
      }
      f4 u[4] = {z, z, z, z};
#pragma unroll
      for (int kb = 0; kb < 7; ++kb)
#pragma unroll
        for (int hh = 0; hh < 4; ++hh) u[hh] = smf<0>(A[kb], W[hh][kb], u[hh], idx);
      u8v Yu;
#pragma unroll
      for (int p = 0; p < 2; ++p) {
        Yu[4 * p + 0] = pk(u[2 * p][0], u[2 * p][1]);
        Yu[4 * p + 1] = pk(u[2 * p + 1][0], u[2 * p + 1][1]);
        Yu[4 * p + 2] = pk(u[2 * p][2], u[2 * p][3]);
        Yu[4 * p + 3] = pk(u[2 * p + 1][2], u[2 * p + 1][3]);
      }
      if (last) {
        asm volatile("" : "+v"(Yu));
        RTT(5);
        tail_wait(tt + c.ot);
        RTT(6);
        for (int sp = 0; sp < SPIN_MAX; ++sp) {
          cbar();
          u32 m = 0;
#pragma unroll
          for (int i = 0; i < 12; ++i) px[i] = ld16c(c.rw, pxo[i % 3] + (i / 3) * 640, so);
#pragma unroll
          for (int g = 0; g < 4; ++g) {
            const f4 o = smf<0>(px[3 * g + 2], Yu, smf<0>(px[3 * g + 1], Y[1], smf<0>(px[3 * g], Y[0], z, idx), idx), idx);
#pragma unroll
            for (int i = 3 * g; i < 3 * g + 3; ++i) m = umax3(umax3(m, px[i][0], px[i][1]), px[i][2], px[i][3]);
#pragma unroll
            for (int r = 0; r < 4; ++r) *(float*)(L + L_RO + g * 5120 + wr + r * 80) = o[r];
            __builtin_amdgcn_sched_barrier(0);
          }
          if (!any_sent(m)) break;
        }
        RTT(7);
      } else {
      f4 o[4];
#pragma unroll
      for (int g = 0; g < 4; ++g) o[g] = smf<0>(px[3 * g], Y[0], z, idx);
#pragma unroll
      for (int g = 0; g < 4; ++g) o[g] = smf<0>(px[3 * g + 1], Y[1], o[g], idx);
#pragma unroll
      for (int g = 0; g < 4; ++g) o[g] = smf<0>(px[3 * g + 2], Yu, o[g], idx);
#pragma unroll
      for (int g = 0; g < 4; ++g)
#pragma unroll
        for (int r = 0; r < 4; ++r) *(float*)(L + L_RO + g * 5120 + wr + r * 80) = o[g][r];
      }
    }
    lds_bar();
    if (s >= 2 * NO) {
      f2 v[8];
#pragma unroll
      for (int p = 0; p < 8; ++p) v[p] = *(f2*)(L + rdo + p * 640);
      f2 sv = ((v[0] + v[1]) + (v[2] + v[3])) + ((v[4] + v[5]) + (v[6] + v[7]));
      __builtin_amdgcn_raw_buffer_store_b32(pk(sv[0], sv[1]), c.ro, sto, (u32)(s - 2 * NO) * 32u * 1792u, OSC);
    }
    Y[0] = Y2[0]; Y[1] = Y2[1]; Y2[0] = Yn[0]; Y2[1] = Yn[1];
  }
}

DEVI void dma16w(rsrc_t r, u32 ldsa, u32 v, u32 so) {
  asm volatile("s_mov_b32 m0, %0\n\ts_nop 0\n\tbuffer_load_dwordx4 %1, %2, %3 offen sc1 lds" :: "s"(ldsa), "v"(v), "s"(r), "s"(so) : "memory");
}
DEVI void role_X(const Ctx& c, int h, int par) {
  const int l = lane_id(), w = c.w, n = l & 15, q = l >> 4;
  char* L = c.lds;
  constexpr int L_W6 = 0, L_SC = 57344, L_X = L_SC + 26624;
  const u32 xo = (u32)(16 * (w >> 1) + n) * 896u + (w & 1) * 448 + q * 16;
  const u32 xl = lds_addr(L) + L_X + w * 7168;
#pragma unroll
  for (int i = 0; i < 7; ++i) dma16(c.rx, xl + i * 1024, xo, (u32)par * 28672u + i * 64);
  wait_pieces(c, (u32)(h * 8 + l * 4 + w) * 128u, l < 2);
  for (int t = 0; t < XD0 + par * XD1; ++t) __builtin_amdgcn_s_sleep(8);
#pragma unroll
  for (int i = 0; i < 14; ++i) dma16w(c.rw, lds_addr(L) + L_W6 + (w * 14 + i) * 1024, l * 16, OFF_WA + (u32)(((h * 4 + w) * 7 + 6) * 14 + i) * 1024u);
  u4 W[6][14];
#pragma unroll
  for (int tt = 0; tt < 6; ++tt)
#pragma unroll
    for (int ks = 0; ks < 14; ++ks) W[tt][ks] = ld16w(c.rw, l * 16, OFF_WA + (u32)(((h * 4 + w) * 7 + tt) * 14 + ks) * 1024u);
  int kc[4];
  kc[0] = 112 * w + 16 * q; kc[1] = kc[0] + 8; kc[2] = 112 * w + 64 + 8 * q; kc[3] = 112 * w + 96 + 8 * (q >> 1);
  u32 ko[4], mo[4];
#pragma unroll
  for (int i = 0; i < 4; ++i) { ko[i] = ((kc[i] >> 5) * 64 + ((kc[i] >> 3) & 3) * 16 + n) * 16; mo[i] = (kc[i] >> 4) * 256 + (n & 7) * 32 + (kc[i] & 8); }
  u32 pw0 = 0;
  const f4 z = {0, 0, 0, 0};
  const u32 wsc = L_SC + (w * 32 + n) * 208;
  const int row = 8 * w + (l >> 3), sl = l & 7;
  const u32 rsc = L_SC + row * 208;
  u32 xb = 0;
  asm volatile("s_waitcnt vmcnt(0)" ::: "memory");
  for (int s = par; s < NSEG; s += NX) {
#pragma unroll
    for (int tt = 0; tt < 6; ++tt)
#pragma unroll
      for (int ks = 0; ks < 14; ++ks) { if (tt * 14 + ks < 54) asm volatile("" : "+a"(W[tt][ks])); else asm volatile("" : "+v"(W[tt][ks])); }
    lds_bar();
    char* X = L + L_X + xb;
    xb ^= 28672u;
    if (s + NX < NSEG) {
#pragma unroll
      for (int i = 0; i < 7; ++i) dma16(c.rx, xl + xb + i * 1024, xo, (u32)(s + NX) * 28672u + i * 64);
      st16c(c.rw, l < 40 ? (u32)(w * 640 + l * 16) : 0x80000000u, OFF_PX + (u32)(s + NX) * 40960u + (u32)h * 2560u, (u4){~0u, ~0u, ~0u, ~0u});
    }
    u4 T[2][4];
    u4 b = *(u4*)(X + l * 16), w6 = *(u4*)(L + L_W6 + (w * 14) * 1024 + l * 16);
#pragma unroll
    for (int mt = 0; mt < 2; ++mt) {
      f4 acc[7];
#pragma unroll
      for (int tt = 0; tt < 7; ++tt) acc[tt] = z;
#pragma unroll
      for (int ks = 0; ks < 14; ++ks) {
        u4 bn = b, wn = w6;
        if (14 * mt + ks < 27) { bn = *(u4*)(X + (14 * mt + ks + 1) * 1024 + l * 16); wn = *(u4*)(L + L_W6 + (w * 14 + (ks + 1) % 14) * 1024 + l * 16); }
        __builtin_amdgcn_sched_barrier(0);
#pragma unroll
        for (int tt = 0; tt < 6; ++tt) acc[tt] = mfma(W[tt][ks], b, acc[tt]);
        acc[6] = mfma(w6, b, acc[6]);
        __builtin_amdgcn_sched_barrier(0);
        b = bn; w6 = wn;
      }
#pragma unroll
      for (int p = 0; p < 3; ++p)
        T[mt][p] = (u4){pk(acc[2 * p][0], acc[2 * p][1]), pk(acc[2 * p + 1][0], acc[2 * p + 1][1]), pk(acc[2 * p][2], acc[2 * p][3]), pk(acc[2 * p + 1][2], acc[2 * p + 1][3])};
      u32 zl = pk(acc[6][0], acc[6][1]), zh = pk(acc[6][2], acc[6][3]);
      T[mt][3] = (q & 1) ? (u4){0, zl, 0, zh} : (u4){zl, 0, zh, 0};
    }
    f4 s00 = z, s01 = z, s11 = z;
#pragma unroll
    for (int i = 0; i < 4; ++i) {
      u4 k0 = *(u4*)(X + ko[i]), k1 = *(u4*)(X + ko[i] + 14 * 1024);
      s00 = mfma(k0, T[0][i], s00); s01 = mfma(k0, T[1][i], s01); s11 = mfma(k1, T[1][i], s11);
    }
    *(f4*)(L + wsc + (8 + 4 * q) * 4) = s00;
    *(f4*)(L + wsc + 16 * 208 + (8 + 4 * q) * 4) = s01;
    *(f4*)(L + wsc + 16 * 208 + (24 + 4 * q) * 4) = s11;
    lds_bar();
    f4 sx = z;
#pragma unroll
    for (int p = 0; p < 4; ++p) sx += *(f4*)(L + rsc + p * 6656 + 32 + sl * 16);
    const float NEG = -1.0e9f * 1.4426950408889634f;
#pragma unroll
    for (int r = 0; r < 4; ++r) sx[r] = (4 * sl + r <= row) ? sx[r] : NEG;
    float m = fmaxf(fmaxf(sx[0], sx[1]), fmaxf(sx[2], sx[3]));
    asm volatile("" : "+v"(sx), "+v"(m));
    u4 M[4];
    if (s == par) pw0 = wait_go(c);
    for (int sp = 0; sp < SPIN_MAX && s < XDIR && s > 0; ++sp) {
      cbar();
      u32 m = __builtin_amdgcn_raw_buffer_load_b32(c.rw, l == 0 ? XFLAG : 0x80000000u, OFF_P + pslot(s) + ((pw0 >> 1) & 8192u), SC1);
      if (!any_sent(m)) break;
      __builtin_amdgcn_s_sleep(XPOLL);
    }
    if (s == NSEG - 1) { const u32 tt = tail_time(c, h, s, pw0); RTT(5); tail_wait(tt + c.xt - 1024u); RTT(6); }
    for (int sp = 0; sp < SPIN_MAX; ++sp) {
      cbar();
      u32 m = 0;
      const u32 sm = OFF_MEMA + (u32)s * 16384u + (pw0 & 8192u);
#pragma unroll
      for (int i = 0; i < 4; ++i) {
        u2 a = __builtin_amdgcn_raw_buffer_load_b64(c.rw, mo[i], sm, SC1), b = __builtin_amdgcn_raw_buffer_load_b64(c.rw, mo[i] + 16, sm, SC1);
        M[i] = (u4){a[0], b[0], a[1], b[1]};
      }
#pragma unroll
      for (int i = 0; i < 4; ++i) m = umax3(umax3(m, M[i][0], M[i][1]), M[i][2], M[i][3]);
      if (!any_sent(m)) break;
      __builtin_amdgcn_s_sleep(4);
    }
    if (s == NSEG - 1) RTT(7);
    f4 m0 = z, m1 = z;
#pragma unroll
    for (int i = 0; i < 4; ++i) { m0 = mfma(M[i], T[0][i], m0); m1 = mfma(M[i], T[1][i], m1); }
    const u32 pm = (q < 2 ? 4 * q : 32 + 4 * q) * 4;
    *(f4*)(L + wsc + pm) = m0;
    *(f4*)(L + wsc + 16 * 208 + pm) = m1;
    lds_bar();
    float sp4[4];
#pragma unroll
    for (int p = 0; p < 4; ++p) sp4[p] = *(float*)(L + rsc + p * 6656 + sl * 4);
    __builtin_amdgcn_sched_barrier(0);
    const float sm = ((sp4[0] + sp4[1]) + sp4[2]) + sp4[3];
    m = fmaxf(m, sm);
    m = fmaxf(m, dppf<0xB1>(m)); m = fmaxf(m, dppf<0x4E>(m)); m = fmaxf(m, dppf<0x141>(m));
    float e[4], em = __builtin_amdgcn_exp2f(sm - m);
#pragma unroll
    for (int r = 0; r < 4; ++r) e[r] = __builtin_amdgcn_exp2f(sx[r] - m);
    float t = (e[0] + e[1]) + (e[2] + e[3]) + em;
    t += dppf<0xB1>(t); t += dppf<0x4E>(t); t += dppf<0x141>(t);
    float inv = __builtin_amdgcn_rcpf(t);
    em *= inv;
    float en = dppf<0xB1>(em);
    const u32 so = OFF_PX + (u32)s * 40960u + (u32)h * 2560u;
    st8c(c.rw, w * 640 + (sl >> 1) * 128 + (l >> 3) * 16 + (sl & 1) * 8, so, (u2){nosent(pk(e[0] * inv, e[1] * inv)), nosent(pk(e[2] * inv, e[3] * inv))});
    st4c(c.rw, (sl & 1) ? 0x80000000u : (u32)(w * 640 + 512 + (l >> 3) * 16 + sl * 2), so, nosent(pk(em, en)));
  }
}

#define NP2 5
#define CDEL 2
#define NPROBE 4
DEVI void role_C(const Ctx& c) {
  const int l = lane_id(), w = c.w;
  u32* L = (u32*)c.lds;
  const u32 vo = l == 0 ? 0u : 0x80000000u;
  {
    u32 b0 = 0xFFFFFFFFu, b1 = 0xFFFFFFFFu;
    const u32 so = ((w & 1) ? OFF_P : OFF_MEMA) + (u32)(2 + (w >> 1)) * 16384u + 8064u;
    for (int i = 0; i < NPROBE; ++i) {
      asm volatile("s_waitcnt vmcnt(0) lgkmcnt(0)" ::: "memory");
      u32 t0 = (u32)__builtin_amdgcn_s_memtime();
      st4c(c.rw, vo, so, 0u);
      asm volatile("s_waitcnt vmcnt(0)" ::: "memory");
      u32 t1 = (u32)__builtin_amdgcn_s_memtime();
      st4c(c.rw, vo, so + 8192u, 0u);
      asm volatile("s_waitcnt vmcnt(0)" ::: "memory");
      u32 t2 = (u32)__builtin_amdgcn_s_memtime();
      b0 = min(b0, t1 - t0); b1 = min(b1, t2 - t1);
    }
    L[8 + 2 * w] = b0; L[9 + 2 * w] = b1;
    wait_pieces(c, (u32)(w * 64 + l) * 128u, true);
    if (w == 2) wait_pieces(c, (u32)(256 + l) * 128u, l < NI);
  }
  lds_bar();
  const u32 pm = L[9] + L[13] < L[8] + L[12] ? 8192u : 0u, pp = L[11] + L[15] < L[10] + L[14] ? 8192u : 0u;
  if (w == 0) st8c(c.rw, vo, OFF_PW, (u2){c.e0, pm | pp << 1});
  {
    for (int t = 0; t < CDEL; ++t) __builtin_amdgcn_s_sleep(32);
    const u32 so = uni(((w & 1) ? OFF_P + pp : OFF_MEMA + pm) + 7168u + (w >> 1) * 65536u);
    int d = 0, pr = 0;
#pragma nounroll
    for (int i = 0; i < 2 * NP2; ++i) {
      unsigned long long t0, t1; u32 x;
      asm volatile("s_memtime %1\n\tbuffer_load_dword %0, %3, %4, %5 offen sc1\n\ts_waitcnt vmcnt(0)\n\ts_memtime %2\n\ts_waitcnt lgkmcnt(0)"
                   : "=v"(x), "=&s"(t0), "=&s"(t1) : "v"(vo), "s"(c.rw), "s"(so + (u32)((i >> 1) & 7) * 128u + (u32)(i >> 4) * 32768u + (u32)(i & 1) * 16384u) : "memory");
      const int t = (int)((u32)t1 - (u32)t0);
      if (i & 1) d += max(-96, min(96, t - pr));
      pr = t;
    }
    L[2 + w] = d;
  }
  lds_bar();
  if (w == 0) {
    const int dm = L[2] + L[4], dp = L[3] + L[5], sm = l + 2, sp = l + 1;
    const bool am = (((sm ^ (sm >> 5)) & 1) ? dm : -dm) > 32 * NP2, ap = ((((PH2 == 16384 ? 0 : sp) ^ (sp >> 4)) & 1) ? -dp : dp) > 32 * NP2, an = abs(dp) > 32 * NP2;
    st4c(c.rw, l < 63 ? pslot(l) + 5184u : 0x80000000u, OFF_P + pp, ~(((am ? c.up & 255 : 0) + (ap ? c.up >> 8 & 255 : (an ? c.up >> 16 : 0))) | (ap ? c.dn << 8 : 0)));
  }
}

DEVI float phi(float t) { return (t > 0.f ? t + 1.f : __expf(t)) + 1e-6f; }

DEVI void role_A(const Ctx& c, rsrc_t ra, int cu, const void* wqkv, const void* wo) {
  const int l = lane_id(), w = c.w, n = l & 15, q = l >> 4;
  char* L = c.lds;
  constexpr int L_X = 0, L_O = 57344;
  const f4 z = {0, 0, 0, 0};
  {
    for (int t = 0; t < ADEL; ++t) __builtin_amdgcn_s_sleep(8);
    const bool isq = cu < 24;
    const rsrc_t rp = mk(isq ? wqkv : wo, isq ? 448u * 1344u * 2u : 448u * 448u * 2u);
    for (int r = 0; r < 2; ++r) {
      u32 pv[7][8];
      const int f0 = (cu * 8 + w * 2 + r) * 7;
#pragma unroll
      for (int i = 0; i < 7; ++i) {
        const int f = f0 + i, ks = f % 14, t = f / 14, dd = 16 * (t & 1) + n, t2 = f - 1344;
        const u32 oq = (u32)((32 * ks + 8 * q) * 1344 + (t >> 5) * 448 + ((t >> 1) & 15) * 28 + dd) * 2u | (dd < 28 ? 0u : 0x80000000u);
        const u32 oo = (u32)(((t2 & 15) * 28 + 4 * q) * 448 + 16 * (t2 >> 4) + n) * 2u;
        const u32 o1 = isq ? oq : oo, o2 = isq ? oq : (q < 3 ? oo : 0x80000000u);
#pragma unroll
        for (int j = 0; j < 8; ++j) pv[i][j] = __builtin_amdgcn_raw_buffer_load_b16(rp, j < 4 ? o1 : o2, isq ? j * 2688 : (16 * (j >> 2) + (j & 3)) * 896, 0);
      }
#pragma unroll
      for (int i = 0; i < 7; ++i) {
        u4 o = {pv[i][0] | pv[i][1] << 16, pv[i][2] | pv[i][3] << 16, pv[i][4] | pv[i][5] << 16, pv[i][6] | pv[i][7] << 16};
        st16c(c.rw, l * 16, OFF_WQKV + (u32)(f0 + i) * 1024u, o);
      }
    }
    done2(c, 64 + cu);
  }
  {
    u4 xr[14];
#pragma unroll
    for (int i = 0; i < 14; ++i) xr[i] = ld16(ra, (u32)(16 * w + n) * 896u + i * 64 + q * 16, (u32)cu * 64u * 896u);
#pragma unroll
    for (int i = 0; i < 14; ++i) *(u4*)(L + L_X + (w * 14 + i) * 1024 + l * 16) = xr[i];
  }
  wait_go(c);
  wait_pieces(c, (u32)(320 + l) * 128u, l < 32);
  __builtin_amdgcn_fence(__ATOMIC_ACQUIRE, "agent");
  lds_bar();
  u4 pq[4][4];
#pragma unroll
  for (int hh = 0; hh < 4; ++hh) {
    const int h = 4 * w + hh;
    f4 aq[4][2], ak[4][2], av[4][2];
#pragma unroll
    for (int mt = 0; mt < 4; ++mt)
#pragma unroll
      for (int ct = 0; ct < 2; ++ct) aq[mt][ct] = ak[mt][ct] = av[mt][ct] = z;
#pragma unroll 2
    for (int ks = 0; ks < 14; ++ks) {
      u4 wf[3][2];
#pragma unroll
      for (int m3 = 0; m3 < 3; ++m3)
#pragma unroll
        for (int ct = 0; ct < 2; ++ct) wf[m3][ct] = ld16(c.rw, l * 16, OFF_WQKV + (u32)(((m3 * 16 + h) * 2 + ct) * 14 + ks) * 1024u);
#pragma unroll
      for (int mt = 0; mt < 4; ++mt) {
        u4 x = *(u4*)(L + L_X + (mt * 14 + ks) * 1024 + l * 16);
#pragma unroll
        for (int ct = 0; ct < 2; ++ct) {
          aq[mt][ct] = mfma(wf[0][ct], x, aq[mt][ct]);
          ak[mt][ct] = mfma(x, wf[1][ct], ak[mt][ct]);
          av[mt][ct] = mfma(x, wf[2][ct], av[mt][ct]);
        }
      }
    }
    u4 fk[2][2], fv[2][2];
#pragma unroll
    for (int ct = 0; ct < 2; ++ct)
#pragma unroll
      for (int ts = 0; ts < 2; ++ts) {
        f4 a = ak[2 * ts][ct], b = ak[2 * ts + 1][ct];
        fk[ct][ts] = (u4){pk(phi(a[0]), phi(a[1])), pk(phi(a[2]), phi(a[3])), pk(phi(b[0]), phi(b[1])), pk(phi(b[2]), phi(b[3]))};
        a = av[2 * ts][ct]; b = av[2 * ts + 1][ct];
        fv[ct][ts] = (u4){pk(a[0], a[1]), pk(a[2], a[3]), pk(b[0], b[1]), pk(b[2], b[3])};
        if (ct == 1 && n == 12) fv[ct][ts] = (u4){0x3F803F80u, 0x3F803F80u, 0x3F803F80u, 0x3F803F80u};
      }
#pragma unroll
    for (int et = 0; et < 2; ++et)
#pragma unroll
      for (int dt = 0; dt < 2; ++dt) {
        f4 kv = mfma(fk[dt][0], fv[et][0], z);
        kv = mfma(fk[dt][1], fv[et][1], kv);
        st16c(c.rw, l * 16, OFF_KVP + (u32)(((cu * 16 + h) * 2 + et) * 2 + dt) * 1024u, __builtin_bit_cast(u4, kv));
      }
#pragma unroll
    for (int mt = 0; mt < 4; ++mt) {
      f4 a = aq[mt][0], b = aq[mt][1];
      u4 t = {pk(phi(a[0]), phi(a[1])), pk(phi(a[2]), phi(a[3])), pk(phi(b[0]), phi(b[1])), pk(phi(b[2]), phi(b[3]))};
      if (q == 3) { t[2] = 0; t[3] = 0; }
      pq[hh][mt] = t;
    }
  }
  asm volatile("s_waitcnt vmcnt(0)\n\ts_barrier" ::: "memory");
  if (w == 0) st4c(c.rw, l == 0 ? (u32)cu * 128u : 0x80000000u, OFF_FLAG, 1u);
  for (int sp = 0; sp < SPIN_MAX; ++sp) {
    cbar();
    u32 f = __builtin_amdgcn_raw_buffer_load_b32(c.rw, (l & 31) * 128, OFF_FLAG, SC1);
    if (!any_sent(f)) break;
    __builtin_amdgcn_s_sleep(8);
  }
  cbar();
  {
    f4 s0 = z, s1 = z;
#pragma unroll
    for (int i = 0; i < 8; ++i) {
      u32 so = OFF_KVP + (u32)((8 * w + i) * 32 + cu) * 2048u;
      s0 += __builtin_bit_cast(f4, ld16c(c.rw, l * 16, so));
      s1 += __builtin_bit_cast(f4, ld16c(c.rw, l * 16 + 1024, so));
    }
    *(f4*)(L + L_X + (2 * w) * 1024 + l * 16) = s0;
    *(f4*)(L + L_X + (2 * w + 1) * 1024 + l * 16) = s1;
    lds_bar();
    if (w == 0) {
      s0 = z; s1 = z;
#pragma unroll
      for (int i = 0; i < 4; ++i) { s0 += *(f4*)(L + L_X + (2 * i) * 1024 + l * 16); s1 += *(f4*)(L + L_X + (2 * i + 1) * 1024 + l * 16); }
      st16c(c.rw, l * 16, OFF_KVF + (u32)cu * 1024u, (u4){nosent(pk(s0[0], s0[1])), nosent(pk(s0[2], s0[3])), nosent(pk(s1[0], s1[1])), nosent(pk(s1[2], s1[3]))});
    }
  }
  u4 kf[8];
  for (int sp = 0; sp < SPIN_MAX; ++sp) {
    cbar();
    u32 m = 0;
#pragma unroll
    for (int i = 0; i < 8; ++i) kf[i] = ld16c(c.rw, l * 16 + i * 1024, OFF_KVF + (u32)w * 8192u);
#pragma unroll
    for (int i = 0; i < 8; ++i) m = umax3(umax3(m, kf[i][0], kf[i][1]), kf[i][2], kf[i][3]);
    if (!any_sent(m)) break;
    __builtin_amdgcn_s_sleep(8);
  }
#pragma unroll
  for (int hh = 0; hh < 4; ++hh)
#pragma unroll
    for (int mt = 0; mt < 4; ++mt) {
      f4 a = mfma(kf[2 * hh], pq[hh][mt], z), b = mfma(kf[2 * hh + 1], pq[hh][mt], z);
      float den = __shfl(b[0], 48 + n) + 1e-6f;
      float inv = 1.0f / den;
      a *= inv; b *= inv;
      u4 t = {pk(a[0], a[1]), pk(a[2], a[3]), pk(b[0], b[1]), pk(b[2], b[3])};
      if (q == 3) { t[2] = 0; t[3] = 0; }
      *(u4*)(L + L_O + ((4 * w + hh) * 4 + mt) * 1024 + l * 16) = t;
    }
  lds_bar();
  f4 acc[7][4];
#pragma unroll
  for (int t = 0; t < 7; ++t)
#pragma unroll
    for (int mt = 0; mt < 4; ++mt) acc[t][mt] = z;
#pragma unroll 2
  for (int h = 0; h < 16; ++h) {
    u4 wf[7];
#pragma unroll
    for (int t = 0; t < 7; ++t) wf[t] = ld16(c.rw, l * 16, OFF_WO + (u32)((7 * w + t) * 16 + h) * 1024u);
#pragma unroll
    for (int mt = 0; mt < 4; ++mt) {
      u4 o = *(u4*)(L + L_O + (h * 4 + mt) * 1024 + l * 16);
#pragma unroll
      for (int t = 0; t < 7; ++t) acc[t][mt] = mfma(wf[t], o, acc[t][mt]);
    }
  }
#pragma unroll
  for (int mt = 0; mt < 4; ++mt)
#pragma unroll
    for (int t = 0; t < 7; ++t) {
      f4 a = acc[t][mt];
      __builtin_amdgcn_raw_buffer_store_b64((u2){pk(a[0], a[1]), pk(a[2], a[3])}, c.ro, (u32)(16 * mt + n) * 1792u + (7 * w + t) * 32 + q * 8, (u32)cu * 64u * 1792u, ASC);
    }
}

#define PARM 0
#define PARP 0

#define TARG
#define TVAL
constexpr int g_tune[6] = {660520, 45, 2240, 200, 1378, 176948524};
__global__ void __launch_bounds__(256, 1)
mainq(const void* ax, const void* bx, void* out, char* ws, const void* wqkv, const void* wo, const void* mem0, const void* Wq, const void* Wk, const void* Wv, const void* Wo TARG) {
  extern __shared__ __attribute__((aligned(16))) char lds[];
  asm volatile("" : "+s"(ws), "+s"(Wq), "+s"(Wk), "+s"(Wo));
  Ctx c;
  c.rw = mk(ws, WS_USED);
  c.rx = mk(bx, 2048u * 896u);
  c.ro = mk(out, 2048u * 1792u);
  c.lds = lds;
  c.w = __builtin_amdgcn_readfirstlane(threadIdx.x >> 6);
  c.d0 = g_tune[2]; c.d1 = g_tune[3]; c.d2 = g_tune[4]; c.up = g_tune[0]; c.dn = g_tune[1]; c.xt = g_tune[5] & 0xffff; c.ot = g_tune[5] >> 16;
  int role, a0, a1;
  produce(c, Wq, Wk, Wv, Wo, role, a0, a1);
  if (__builtin_expect(c.isS == 1, 1)) role_S(c, a0);
  if (__builtin_expect(c.isV == 0, 1)) role_V(c, a0);
  if (c.isO == 2) { role_O(c, a0, a1); __builtin_amdgcn_endpgm(); }
  if (c.isX == 3) { role_X(c, a0, a1); __builtin_amdgcn_endpgm(); }
  if (role == 5) role_C(c);
  if (role < 0 && a0 < NI) role_I(c, a0, mem0);
  if (role == 4) role_A(c, mk(ax, 2048u * 896u), a0, wqkv, wo);
}

extern "C" void kernel_launch(void* const* d_in, const int* in_sizes, int n_in, void* d_out, int out_size, void* d_ws, size_t ws_size, hipStream_t stream) {
  char* ws = (char*)(((uintptr_t)d_ws + MB2 - 1) & ~(uintptr_t)(MB2 - 1));
  (void)hipFuncSetAttribute((const void*)mainq, hipFuncAttributeMaxDynamicSharedMemorySize, LDS_BYTES);
  mainq<<<256, 256, LDS_BYTES, stream>>>(d_in[0], d_in[3], d_out, ws, d_in[1], d_in[2], d_in[4], d_in[5], d_in[6], d_in[7], d_in[8] TVAL);
}
```
